```python
import torch

def _softmax(x, axis=-1):
    return torch.softmax(x, dim=axis)
def _gelu(x):
    return torch.nn.functional.gelu(x, approximate="tanh")
def _silu(x):
    return torch.nn.functional.silu(x)
def _sigmoid(x):
    return torch.sigmoid(x)
def _layernorm(x, axis=-1, eps=1e-5):
    m = x.mean(dim=axis, keepdim=True)
    v = x.var(dim=axis, keepdim=True, unbiased=False)
    return (x - m) / torch.sqrt(v + eps)
def _scatter_add(tgt, idx, src, axis=0):
    return tgt.clone().index_add_(axis, idx, src)
def _segment_sum(src, seg, n):
    out = torch.zeros((n,) + src.shape[1:], dtype=src.dtype)
    return out.index_add_(0, seg, src)
def _pad(x, pw):
    flat = tuple(v for pair in reversed(pw) for v in pair)
    return torch.nn.functional.pad(x, flat)

kernel_name = 'interleave_layers__gqa_block_cached__dense_activation_gelu__decode'

def setup_inputs():

    _w = (lambda a: (lambda t: t.to(torch.bfloat16) if t.is_floating_point() else t)(torch.as_tensor(a)))
    def _A_setup():
        B, Sq, D = 2, 1, 512
        Hq, Hkv, Sk, Dh = 32, 32, 4096, 128
        assert Hq % Hkv == 0, (Hq, Hkv)
        return {
            "x": _w((lambda *s: torch.randn(*s, dtype=torch.bfloat16) * 0.05)(B, Sq, D)),
            "k_cache": _w((lambda *s: torch.randn(*s, dtype=torch.bfloat16) * 0.8)(B, Hkv, Sk, Dh)),
            "v_cache": _w((lambda *s: torch.randn(*s, dtype=torch.bfloat16) * 1.6)(B, Hkv, Sk, Dh)),
            "wq": _w((lambda *s: torch.randn(*s, dtype=torch.bfloat16) * 0.8)(D, Hq * Dh) * (D ** -0.5)),
            "wo": _w((lambda *s: torch.randn(*s, dtype=torch.bfloat16) * 1.6)(Hq * Dh, D) * ((Hq * Dh) ** -0.5)),
        }
    _A_in = _A_setup()

    def _B_setup():
        B, S, D, F = 2, 1, 512, 1376
        return {
            "x": _w((lambda *s: torch.randn(*s, dtype=torch.bfloat16) * 0.05)(B, S, D)),
            "w1": _w((lambda *s: torch.randn(*s, dtype=torch.bfloat16) * 0.4)(D, F) * (D ** -0.5)),
            "b1": _w((lambda *s: torch.randn(*s, dtype=torch.bfloat16) * 0.4)(F) * 0.01),
            "w2": _w((lambda *s: torch.randn(*s, dtype=torch.bfloat16) * 0.4)(F, D) * (F ** -0.5)),
            "b2": _w((lambda *s: torch.randn(*s, dtype=torch.bfloat16) * 0.4)(D) * 0.01),
        }
    _B_in = _B_setup()

    return {
        "A_x": _A_in["x"],
        "A_k_cache": _A_in["k_cache"],
        "A_v_cache": _A_in["v_cache"],
        "A_wq": _A_in["wq"],
        "A_wo": _A_in["wo"],
        "B_x": _B_in["x"],
        "B_w1": _B_in["w1"],
        "B_b1": _B_in["b1"],
        "B_w2": _B_in["w2"],
        "B_b2": _B_in["b2"],
    }

def reference(A_x, A_k_cache, A_v_cache, A_wq, A_wo, B_x, B_w1, B_b1, B_w2, B_b2):
    def _A_ref(x, k_cache, v_cache, wq, wo):
        B, Sq, D = x.shape
        Hq, Dh = 32, 128
        Hkv = k_cache.shape[1]
        rep = Hq // Hkv
        q = (lambda x, *d: x.permute(*d))((x @ wq).reshape(B, Sq, Hq, Dh), 0, 2, 1, 3)
        k = (lambda x, n, axis: torch.repeat_interleave(x, n, dim=axis))(k_cache, rep, axis=1)
        v = (lambda x, n, axis: torch.repeat_interleave(x, n, dim=axis))(v_cache, rep, axis=1)
        scores = torch.einsum("bhqd,bhkd->bhqk", q, k) * (Dh ** -0.5)
        p = _softmax(scores, axis=-1)
        o = torch.einsum("bhqk,bhkd->bhqd", p, v)
        return (lambda x, *d: x.permute(*d))(o, 0, 2, 1, 3).reshape(B, Sq, Hq * Dh) @ wo

    def _B_ref(x, w1, b1, w2, b2):
        h = torch.einsum("bsd,df->bsf", x, w1) + b1
        h = _gelu(h)
        return torch.einsum("bsf,fd->bsd", h, w2) + b2

    x = A_x
    x = x + _A_ref(_layernorm(x, axis=-1), A_k_cache, A_v_cache, A_wq, A_wo)
    x = x + _B_ref(_layernorm(x, axis=-1), B_w1, B_b1, B_w2, B_b2)
    x = x + _A_ref(_layernorm(x, axis=-1), A_k_cache, A_v_cache, A_wq, A_wo)
    x = x + _B_ref(_layernorm(x, axis=-1), B_w1, B_b1, B_w2, B_b2)
    return x
```

```cpp
#include <hip/hip_runtime.h>
#include <hip/hip_bf16.h>
#include <stdint.h>

typedef unsigned short u16;
typedef unsigned u32x4 __attribute__((ext_vector_type(4)));
constexpr int B = 2, D = 512, H = 32, SK = 4096, DH = 128, F = 1376, NS = 4;

__device__ __forceinline__ float bf2f(u16 v) { return __uint_as_float((unsigned)v << 16); }
__device__ __forceinline__ float lo(unsigned v) { return __uint_as_float(v << 16); }
__device__ __forceinline__ float hi(unsigned v) { return __uint_as_float(v & 0xffff0000u); }
__device__ __forceinline__ float wsum(float v) { for (int o = 32; o; o >>= 1) v += __shfl_xor(v, o); return v; }
__device__ __forceinline__ float wmax(float v) { for (int o = 32; o; o >>= 1) v = fmaxf(v, __shfl_xor(v, o)); return v; }

__device__ void layernorm512(const float* x, float* xn, float* red) {
  const int t = threadIdx.x;
  float a = x[t], b = x[t + 256];
  float s = wsum(a + b);
  if ((t & 63) == 0) red[t >> 6] = s;
  __syncthreads();
  float m = (red[0] + red[1] + red[2] + red[3]) * (1.f / D);
  __syncthreads();
  float da = a - m, db = b - m;
  s = wsum(da * da + db * db);
  if ((t & 63) == 0) red[t >> 6] = s;
  __syncthreads();
  float v = (red[0] + red[1] + red[2] + red[3]) * (1.f / D);
  float r = 1.f / sqrtf(v + 1e-5f);
  xn[t] = da * r; xn[t + 256] = db * r;
  __syncthreads();
}

__global__ void k_init(const u16* x, float* xf) { int i = blockIdx.x * 256 + threadIdx.x; xf[i] = bf2f(x[i]); }

__global__ void __launch_bounds__(256) k_attn(const float* xf, const u16* wq, const u16* K, const u16* V, float* part) {
  __shared__ float xn[D], red[8], q[2][DH], sc[1024], ow[4][DH];
  const int blk = blockIdx.x, s = blk & 3, h = (blk >> 2) & 31, b = blk >> 7, t = threadIdx.x, lane = t & 63, w = t >> 6;
  layernorm512(xf + b * D, xn, red);
  { int c = t & 127, half = t >> 7; float acc = 0;
    for (int i = half * 256; i < half * 256 + 256; ++i) acc += xn[i] * bf2f(wq[(size_t)i * (H * DH) + h * DH + c]);
    q[half][c] = acc; }
  __syncthreads();
  float qv[8];
  for (int e = 0; e < 8; ++e) qv[e] = (q[0][(lane & 15) * 8 + e] + q[1][(lane & 15) * 8 + e]) * 0.08838834764831845f;
  const size_t base = ((size_t)(b * H + h) * SK + s * 1024 + w * 256) * DH;
  const u32x4* Kp = (const u32x4*)(K + base) + lane;
  const u32x4* Vp = (const u32x4*)(V + base) + lane;
#pragma unroll 8
  for (int i = 0; i < 64; ++i) {
    u32x4 k = __builtin_nontemporal_load(Kp + i * 64);
    float d = lo(k[0]) * qv[0] + hi(k[0]) * qv[1] + lo(k[1]) * qv[2] + hi(k[1]) * qv[3] + lo(k[2]) * qv[4] + hi(k[2]) * qv[5] + lo(k[3]) * qv[6] + hi(k[3]) * qv[7];
    for (int o = 8; o; o >>= 1) d += __shfl_xor(d, o);
    if ((lane & 15) == 0) sc[w * 256 + i * 4 + (lane >> 4)] = d;
  }
  __syncthreads();
  float m = fmaxf(fmaxf(sc[t], sc[t + 256]), fmaxf(sc[t + 512], sc[t + 768]));
  m = wmax(m);
  if (lane == 0) red[w] = m;
  __syncthreads();
  m = fmaxf(fmaxf(red[0], red[1]), fmaxf(red[2], red[3]));
  float l = 0;
  for (int j = 0; j < 4; ++j) { float p = __expf(sc[t + j * 256] - m); sc[t + j * 256] = p; l += p; }
  l = wsum(l);
  if (lane == 0) red[4 + w] = l;
  __syncthreads();
  l = red[4] + red[5] + red[6] + red[7];
  float acc[8] = {0, 0, 0, 0, 0, 0, 0, 0};
#pragma unroll 8
  for (int i = 0; i < 64; ++i) {
    u32x4 v = __builtin_nontemporal_load(Vp + i * 64);
    float p = sc[w * 256 + i * 4 + (lane >> 4)];
    for (int e = 0; e < 4; ++e) { acc[2 * e] += p * lo(v[e]); acc[2 * e + 1] += p * hi(v[e]); }
  }
  for (int e = 0; e < 8; ++e) { acc[e] += __shfl_xor(acc[e], 16); acc[e] += __shfl_xor(acc[e], 32); }
  if (lane < 16) for (int e = 0; e < 8; ++e) ow[w][lane * 8 + e] = acc[e];
  __syncthreads();
  float* o = part + (size_t)blk * 132;
  if (t < DH) o[t] = ow[0][t] + ow[1][t] + ow[2][t] + ow[3][t];
  if (t == 0) { o[128] = m; o[129] = l; }
}

__global__ void __launch_bounds__(256) k_wo(const float* part, const u16* wo, float* xp) {
  __shared__ float o[2][DH];
  const int h = blockIdx.x >> 2, cq = blockIdx.x & 3, t = threadIdx.x;
  { int b = t >> 7, d = t & 127; const float* p = part + (size_t)((b * H + h) * NS) * 132;
    float m = -INFINITY; for (int s = 0; s < NS; ++s) m = fmaxf(m, p[s * 132 + 128]);
    float l = 0, a = 0; for (int s = 0; s < NS; ++s) { float w = __expf(p[s * 132 + 128] - m); l += w * p[s * 132 + 129]; a += w * p[s * 132 + d]; }
    o[b][d] = a / l; }
  __syncthreads();
  int b = t >> 7, c = cq * 128 + (t & 127); float acc = 0;
  for (int d = 0; d < DH; ++d) acc += o[b][d] * bf2f(wo[(size_t)(h * DH + d) * D + c]);
  xp[(h * B + b) * D + c] = acc;
}
__global__ void k_red(const float* xp, int n, const float* xin, const u16* bias, float* xout, u16* out) {
  int i = blockIdx.x * 256 + threadIdx.x; float a = xin[i];
  if (bias) a += bf2f(bias[i & (D - 1)]);
  for (int k = 0; k < n; ++k) a += xp[k * B * D + i];
  xout[i] = a;
  if (out) { __hip_bfloat16 v = __float2bfloat16(a); out[i] = *(u16*)&v; }
}
__global__ void __launch_bounds__(256) k_ffn(const float* xf, const u16* w1, const u16* b1, const u16* w2, float* xp) {
  __shared__ float xn[D], red[8], hp[8][32], hh[32];
  const int fb = blockIdx.x, b = blockIdx.y, t = threadIdx.x;
  layernorm512(xf + b * D, xn, red);
  { int f = t & 31, kq = t >> 5; float acc = 0;
    for (int i = kq * 64; i < kq * 64 + 64; ++i) acc += xn[i] * bf2f(w1[(size_t)i * F + fb * 32 + f]);
    hp[kq][f] = acc; }
  __syncthreads();
  if (t < 32) { float z = bf2f(b1[fb * 32 + t]); for (int k = 0; k < 8; ++k) z += hp[k][t];
    hh[t] = 0.5f * z * (1.f + tanhf(0.7978845608028654f * (z + 0.044715f * z * z * z))); }
  __syncthreads();
  for (int c = t; c < D; c += 256) { float acc = 0; for (int f = 0; f < 32; ++f) acc += hh[f] * bf2f(w2[(size_t)(fb * 32 + f) * D + c]);
    xp[(fb * B + b) * D + c] = acc; }
}

extern "C" void kernel_launch(void* const* d_in, const int* in_sizes, int n_in, void* d_out, int out_size, void* d_ws, size_t ws_size, hipStream_t stream) {
  const u16* x = (const u16*)d_in[0]; const u16* K = (const u16*)d_in[1]; const u16* V = (const u16*)d_in[2];
  const u16* wq = (const u16*)d_in[3]; const u16* wo = (const u16*)d_in[4];
  const u16* w1 = (const u16*)d_in[6]; const u16* b1 = (const u16*)d_in[7]; const u16* w2 = (const u16*)d_in[8]; const u16* b2 = (const u16*)d_in[9];
  float* ws = (float*)d_ws; float* xa = ws; float* xb = ws + 1024; float* part = ws + 4096; float* xp = ws + 65536;
  k_init<<<4, 256, 0, stream>>>(x, xa);
  for (int l = 0; l < 2; ++l) {
    k_attn<<<256, 256, 0, stream>>>(xa, wq, K, V, part);
    k_wo<<<128, 256, 0, stream>>>(part, wo, xp);
    k_red<<<4, 256, 0, stream>>>(xp, H, xa, nullptr, xb, nullptr);
    k_ffn<<<dim3(43, 2), 256, 0, stream>>>(xb, w1, b1, w2, xp);
    k_red<<<4, 256, 0, stream>>>(xp, 43, xb, b2, xa, l == 1 ? (u16*)d_out : nullptr);
  }
}
```

```cpp
#include <hip/hip_runtime.h>
#include <stdint.h>

#define DEV_BUILD 0

struct Args {
  const void *k, *v, *wq, *ws, *x, *wo, *w1, *w2, *b1, *b2;
  void *out, *dbg;
};

__global__ void __launch_bounds__(256, 1) __attribute__((aligned(4096))) decode_block_kernel(Args args) {
  __shared__ __attribute__((aligned(16))) unsigned char lds[163840];
  asm volatile(
      ".Lk_start:\n"
      "s_load_dwordx16 s[8:23], %0, 0x0\n"
      "s_load_dwordx8 s[24:31], %0, 0x40\n"
      "v_readfirstlane_b32 s56, v0\n"
      "s_getpc_b64 s[52:53]\n"
      ".Lk_pc:\n"
      "v_and_b32 v0, 63, v0\n"
      "s_lshr_b32 s56, s56, 6\n"
      "s_lshl2_add_u32 s57, %1, s56\n"
      "s_and_b32 s53, s53, 0xffff\n"
      "s_mov_b32 s54, .Lk_end-.Lk_pc\n"
      "s_mov_b32 s55, 0x20000\n"
      "s_and_b32 s68, s57, 0x3e0\n"
      "s_lshl_b32 s68, s68, 5\n"
      "s_and_b32 s79, s57, 0x3\n"
      "s_lshl_b32 s79, s79, 8\n"
      "s_add_u32 s68, s68, s79\n"
      "v_lshl_add_u32 v54, v0, 2, s68\n"
      "buffer_load_dword v55, v54, s[52:55], 0 offen \n"
      "s_and_b32 s76, s57, 0xc0\n"
      "s_lshl_b32 s76, s76, 14\n"
      "s_and_b32 s79, s57, 0x3\n"
      "s_lshl_b32 s79, s79, 18\n"
      "s_add_u32 s76, s76, s79\n"
      "s_and_b32 s79, s57, 0x1c\n"
      "s_lshl_b32 s79, s79, 8\n"
      "s_add_u32 s76, s76, s79\n"
      "s_and_b32 s79, s57, 0x300\n"
      "s_add_u32 s76, s76, s79\n"
      "s_and_b32 s79, s57, 0x20\n"
      "s_lshl_b32 s79, s79, 2\n"
      "s_add_u32 s76, s76, s79\n"
      "v_lshrrev_b32 v11, 3, v0\n"
      "v_and_b32 v11, 6, v11\n"
      "v_xor_b32 v52, v11, v0\n"
      "v_and_b32 v52, 7, v52\n"
      "v_lshlrev_b32 v52, 4, v52\n"
      "v_lshl_add_u32 v52, v11, 14, v52\n"
      "v_bfe_u32 v11, v0, 3, 1\n"
      "v_lshl_add_u32 v52, v11, 13, v52\n"
      "s_and_b32 s74, s57, 0x3\n"
      "s_lshl_b32 s74, s74, 12\n"
      "s_add_u32 s74, s74, 0x1a000\n"
      "s_sub_u32 s50, 0x400000, s76\n"
      "s_mov_b32 s51, 0x20000\n"
      "s_and_b32 s68, s57, 0x1c0\n"
      "s_lshl_b32 s68, s68, 8\n"
      "s_and_b32 s79, s57, 0x3f\n"
      "s_lshl_b32 s79, s79, 7\n"
      "s_add_u32 s68, s68, s79\n"
      "s_add_u32 s68, s68, 0x68000\n"
      "s_and_b32 s69, s57, 0x1ff\n"
      "s_lshl_b32 s69, s69, 7\n"
      "s_add_u32 s69, s69, 0x88000\n"
      "s_cmp_lt_u32 s57, 512\n"
      "s_cselect_b32 s68, s68, s69\n"
      "s_and_b32 s69, s57, 0x10\n"
      "s_lshl_b32 s69, s69, 9\n"
      "s_and_b32 s79, s57, 0xf\n"
      "s_lshl_b32 s79, s79, 7\n"
      "s_add_u32 s69, s69, s79\n"
      "s_add_u32 s69, s69, 0x173f80\n"
      "s_cmp_lt_u32 s57, 672\n"
      "s_cselect_b32 s69, s69, 0x40000000\n"
      "s_and_b32 s77, s57, 0x7f\n"
      "s_lshl_b32 s77, s77, 7\n"
      "s_add_u32 s77, s77, 0x16ff80\n"
      "s_cmp_lt_u32 s57, 640\n"
      "s_cselect_b32 s69, s77, s69\n"
      "s_add_u32 s77, s68, 8064\n"
      "s_cmp_lt_u32 s57, 512\n"
      "s_cselect_b32 s69, s77, s69\n"
      "v_lshl_add_u32 v53, v0, 4, s68\n"
      "v_lshl_add_u32 v54, v0, 4, s69\n"
      "s_mov_b64 s[78:79], 0xff\n"
      "v_cndmask_b32 v53, v54, v53, s[78:79]\n"
      "s_mov_b32 s78, 0xffff\n"
      "s_mov_b32 s79, 0x0\n"
      "v_mov_b32 v11, 0x40000000\n"
      "v_cndmask_b32 v53, v11, v53, s[78:79]\n"
      "v_mov_b32 v56, -1\n"
      "v_mov_b32 v57, -1\n"
      "v_mov_b32 v58, -1\n"
      "v_mov_b32 v59, -1\n"
      "s_mov_b32 s42, 4194304\n"
      "s_mov_b32 s43, 0x20000\n"
      "s_mov_b32 m0, s74\n"
      "s_mov_b32 s68, 16384\n"
      "s_waitcnt lgkmcnt(0)\n"
      "s_add_u32 s48, s12, s76\n"
      "s_addc_u32 s49, s13, 0\n"
      "s_and_b32 s49, s49, 0xffff\n"
      "buffer_load_dwordx4 v52, s[48:51], 0 offen nt lds\n"
      "s_add_u32 m0, s74, 1024\n"
      "s_memrealtime s[4:5]\n"
      "buffer_load_dwordx4 v52, s[48:51], s68 offen nt lds\n"
      "s_add_u32 s14, s14, 0x1fffff\n"
      "s_addc_u32 s15, s15, 0\n"
      "s_and_b32 s14, s14, 0xffe00000\n"
      "s_mov_b32 s40, s14\n"
      "s_and_b32 s41, s15, 0xffff\n"
      "buffer_store_dwordx4 v[56:59], v53, s[40:43], 0 offen sc1 nt\n"
      "s_and_b32 s77, s57, 0x80\n"
      "s_lshl_b32 s77, s77, 18\n"
      "s_and_b32 s79, s57, 0x1c\n"
      "s_lshl_b32 s79, s79, 20\n"
      "s_add_u32 s77, s77, s79\n"
      "s_and_b32 s79, s57, 0x300\n"
      "s_lshl_b32 s79, s79, 12\n"
      "s_add_u32 s77, s77, s79\n"
      "s_and_b32 s79, s57, 0x60\n"
      "s_lshl_b32 s79, s79, 13\n"
      "s_add_u32 s77, s77, s79\n"
      "s_and_b32 s79, s57, 0x3\n"
      "s_lshl_b32 s79, s79, 15\n"
      "s_add_u32 s77, s77, s79\n"
      "s_and_b32 s83, s57, 0x20\n"
      "s_lshl_b32 s83, s83, 8\n"
      "s_and_b32 s79, s57, 0x200\n"
      "s_lshl_b32 s79, s79, 5\n"
      "s_add_u32 s83, s83, s79\n"
      "s_and_b32 s79, s57, 0x4\n"
      "s_lshl_b32 s79, s79, 15\n"
      "s_add_u32 s83, s83, s79\n"
      "s_and_b32 s79, s57, 0x80\n"
      "s_lshl_b32 s79, s79, 5\n"
      "s_add_u32 s83, s83, s79\n"
      "s_and_b32 s79, s57, 0x2\n"
      "s_lshl_b32 s79, s79, 10\n"
      "s_add_u32 s83, s83, s79\n"
      "s_add_u32 s84, s8, s77\n"
      "s_addc_u32 s85, s9, 0\n"
      "s_and_b32 s85, s85, 0xffff\n"
      "s_sub_u32 s86, 0x4000000, s77\n"
      "s_mov_b32 s87, 0x20000\n"
      "s_add_u32 s88, s10, s77\n"
      "s_addc_u32 s89, s11, 0\n"
      "s_and_b32 s89, s89, 0xffff\n"
      "s_sub_u32 s90, 0x4000000, s77\n"
      "s_mov_b32 s91, 0x20000\n"
      "s_mov_b32 s44, s16\n"
      "s_and_b32 s45, s17, 0xffff\n"
      "s_mov_b32 s46, 0x800\n"
      "s_mov_b32 s47, 0x20000\n"
      "v_bfe_u32 v11, v0, 1, 3\n"
      "v_lshlrev_b32 v101, 8, v11\n"
      "v_bfe_u32 v11, v0, 4, 2\n"
      "v_lshl_add_u32 v101, v11, 5, v101\n"
      "v_bfe_u32 v11, v0, 0, 1\n"
      "v_lshl_add_u32 v101, v11, 4, v101\n"
      "s_and_b32 s68, s57, 0x2\n"
      "s_lshl_b32 s68, s68, 9\n"
      "v_xor_b32 v101, s68, v101\n"
      "s_mul_i32 s82, s56, 26624\n"
      "v_lshl_add_u32 v2, v0, 4, s82\n"
      "s_movk_i32 s30, 76\n"
      "s_add_u32 s30, s30, 800\n"
      "s_waitcnt lgkmcnt(0)\n"
      "s_mov_b32 s5, s30\n"
      "s_memrealtime s[6:7]\n"
      "s_waitcnt lgkmcnt(0)\n"
      "s_sub_u32 s6, s6, s4\n"
      "s_lshl_b32 s6, s6, 5\n"
      "s_sub_u32 s6, s5, s6\n"
      "s_cmp_lt_i32 s6, 640\n"
      "s_cbranch_scc1 .Lk_pc_1\n"
      "s_sleep 5\n"
      ".Lk_pc_1:\n"
      "s_memrealtime s[6:7]\n"
      "s_waitcnt lgkmcnt(0)\n"
      "s_sub_u32 s6, s6, s4\n"
      "s_lshl_b32 s6, s6, 5\n"
      "s_cmp_lt_i32 s6, s5\n"
      "s_cbranch_scc1 .Lk_pc_1\n"
      "s_add_u32 s5, s5, s30\n"
      "s_add_u32 m0, s74, 2048\n"
      "s_mov_b32 s68, 131072\n"
      "buffer_load_dwordx4 v52, s[48:51], s68 offen nt lds\n"
      "s_add_u32 m0, s74, 3072\n"
      "s_mov_b32 s68, 147456\n"
      "buffer_load_dwordx4 v52, s[48:51], s68 offen nt lds\n"
      "s_and_b32 s68, s57, 0xc0\n"
      "s_lshl_b32 s68, s68, 2\n"
      "v_bfe_u32 v11, v0, 2, 1\n"
      "v_lshl_add_u32 v46, v11, 10, s68\n"
      "v_bfe_u32 v11, v0, 4, 2\n"
      "v_lshl_add_u32 v46, v11, 6, v46\n"
      "v_bfe_u32 v11, v0, 0, 2\n"
      "v_lshl_add_u32 v46, v11, 4, v46\n"
      "s_and_b32 s68, s57, 0x80\n"
      "s_lshl_b32 s68, s68, 3\n"
      "v_lshl_add_u32 v47, v0, 4, s68\n"
      "v_mov_b32 v53, 1544192\n"
      "buffer_load_dwordx2 v[44:45], v53, s[40:43], 0 offen sc1\n"
      "s_cmp_ge_u32 s57, 4\n"
      "s_cbranch_scc1 .Lk_tf_2\n"
      "s_and_b32 s68, s57, 0x3\n"
      "s_lshl_b32 s68, s68, 10\n"
      "s_add_u32 s68, s68, 0x178000\n"
      "v_lshl_add_u32 v53, v0, 4, s68\n"
      "buffer_load_dwordx4 v[60:63], v53, s[40:43], 0 offen sc1\n"
      ".Lk_tf_2:\n"
      "s_and_b32 s68, s57, 0x383\n"
      "s_cmp_lg_u32 s68, 0x380\n"
      "s_cbranch_scc1 .Lk_tp_3\n"
      "s_and_b32 s68, s57, 0x7c\n"
      "s_lshl_b32 s68, s68, 5\n"
      "s_add_u32 s68, s68, 0x1d8000\n"
      "v_bfe_u32 v11, v0, 3, 1\n"
      "v_lshl_add_u32 v53, v11, 13, s68\n"
      "v_bfe_u32 v11, v0, 0, 3\n"
      "v_lshl_add_u32 v53, v11, 4, v53\n"
      "v_bfe_u32 v11, v0, 4, 2\n"
      "v_lshl_add_u32 v53, v11, 29, v53\n"
      "v_mov_b64 v[64:65], 0\n"
      "v_mov_b64 v[66:67], 0\n"
      "buffer_store_dwordx4 v[64:67], v53, s[40:43], 0 offen sc1\n"
      ".Lk_tp_3:\n"
      "buffer_load_dwordx4 v[12:15], v46, s[44:47], 0 offen \n"
      "buffer_load_dwordx4 v[24:27], v47, s[44:47], 0 offen \n"
      "s_and_b32 s76, s57, 0x1c\n"
      "s_lshr_b32 s76, s76, 2\n"
      "s_and_b32 s77, s57, 0x3e0\n"
      "s_lshr_b32 s77, s77, 5\n"
      "s_mul_i32 s68, s76, 12\n"
      "s_add_u32 s68, s68, s77\n"
      "s_sub_u32 s68, s68, 16\n"
      "s_and_b32 s69, s57, 0x1c\n"
      "s_and_b32 s79, s57, 0x60\n"
      "s_lshr_b32 s79, s79, 5\n"
      "s_add_u32 s69, s69, s79\n"
      "s_cmp_lt_u32 s68, 88\n"
      "s_cselect_b32 s76, 1, 3\n"
      "s_cmp_lt_u32 s77, 28\n"
      "s_cselect_b32 s61, s76, 2\n"
      "s_cselect_b32 s75, s68, s69\n"
      "s_cmp_lt_u32 s77, 16\n"
      "s_cselect_b32 s61, 0, s61\n"
      "s_min_u32 s75, s75, 87\n"
      "s_cmp_eq_u32 s61, 0\n"
      "s_cbranch_scc1 .Lk_wf_4\n"
      "s_mov_b32 s52, s24\n"
      "s_and_b32 s53, s25, 0xffff\n"
      "s_mov_b32 s54, 0xac0\n"
      "s_mov_b32 s55, 0x20000\n"
      "s_lshl_b32 s68, s75, 5\n"
      "v_bfe_u32 v11, v0, 4, 2\n"
      "v_lshl_add_u32 v53, v11, 3, s68\n"
      "v_bfe_u32 v11, v0, 0, 2\n"
      "v_lshl_add_u32 v53, v11, 1, v53\n"
      "v_mov_b32 v3, 0\n"
      "buffer_load_short_d16_hi v3, v53, s[52:55], 0 offen\n"
      "s_min_u32 s76, s75, 85\n"
      "s_lshl_b32 s68, s76, 14\n"
      "s_and_b32 s69, s57, 0x3\n"
      "s_lshl_b32 s69, s69, 8\n"
      "s_or_b32 s68, s68, s69\n"
      "s_add_u32 s48, s22, s68\n"
      "s_addc_u32 s49, s23, 0\n"
      "s_and_b32 s49, s49, 0xffff\n"
      "s_sub_u32 s50, 0x158000, s68\n"
      "s_mov_b32 s51, 0x20000\n"
      "s_and_b32 s68, s57, 0x60\n"
      "s_lshr_b32 s68, s68, 3\n"
      "s_and_b32 s79, s57, 0x3\n"
      "s_add_u32 s68, s68, s79\n"
      "s_mul_i32 s68, s68, 88064\n"
      "s_and_b32 s69, s75, 0x7c\n"
      "s_lshl_b32 s69, s69, 5\n"
      "s_add_u32 s68, s68, s69\n"
      "s_add_u32 s52, s20, s68\n"
      "s_addc_u32 s53, s21, 0\n"
      "s_and_b32 s53, s53, 0xffff\n"
      "s_sub_u32 s54, 0x158000, s68\n"
      "s_mov_b32 s55, 0x20000\n"
      "v_bfe_u32 v11, v0, 3, 1\n"
      "v_lshlrev_b32 v97, 0, v11\n"
      "v_bfe_u32 v11, v0, 4, 2\n"
      "v_lshl_add_u32 v97, v11, 2, v97\n"
      "v_mul_u32_u24 v97, 2752, v97\n"
      "v_lshrrev_b32 v11, 3, v0\n"
      "v_and_b32 v11, 6, v11\n"
      "v_xor_b32 v11, v11, v0\n"
      "v_and_b32 v11, 7, v11\n"
      "v_lshl_add_u32 v97, v11, 4, v97\n"
      "v_bfe_u32 v11, v0, 4, 2\n"
      "v_lshlrev_b32 v9, 10, v11\n"
      "v_bfe_u32 v11, v0, 0, 4\n"
      "v_lshl_add_u32 v9, v11, 4, v9\n"
      "s_and_b32 s58, s57, 0x3\n"
      "s_lshl_b32 s58, s58, 12\n"
      "s_add_u32 s58, s58, 0x1e000\n"
      ".Lk_wf_4:\n"
      "s_mov_b32 s16, 0\n"
      "s_cmp_ge_u32 s57, 512\n"
      "s_cbranch_scc1 .Lk_wo_5\n"
      "s_and_b32 s68, s57, 0x1c\n"
      "s_lshl_b32 s68, s68, 17\n"
      "s_and_b32 s79, s57, 0x100\n"
      "s_lshl_b32 s79, s79, 10\n"
      "s_add_u32 s68, s68, s79\n"
      "s_and_b32 s79, s57, 0x3\n"
      "s_lshl_b32 s79, s79, 16\n"
      "s_add_u32 s68, s68, s79\n"
      "s_and_b32 s79, s57, 0xe0\n"
      "s_lshl_b32 s79, s79, 2\n"
      "s_add_u32 s68, s68, s79\n"
      "s_xor_b32 s68, s68, 128\n"
      "s_add_u32 s52, s18, s68\n"
      "s_addc_u32 s53, s19, 0\n"
      "s_and_b32 s53, s53, 0xffff\n"
      "s_sub_u32 s54, 0x400000, s68\n"
      "s_mov_b32 s55, 0x20000\n"
      "v_lshrrev_b32 v11, 3, v0\n"
      "v_and_b32 v11, 6, v11\n"
      "v_xor_b32 v97, v11, v0\n"
      "v_and_b32 v97, 7, v97\n"
      "v_lshlrev_b32 v97, 4, v97\n"
      "v_lshl_add_u32 v97, v11, 11, v97\n"
      "v_bfe_u32 v11, v0, 3, 1\n"
      "v_lshl_add_u32 v97, v11, 10, v97\n"
      "s_and_b32 s58, s57, 0x3\n"
      "s_lshl_b32 s58, s58, 13\n"
      "s_add_u32 s58, s58, 0x1e000\n"
      "s_bfe_u32 s16, s52, 0x30007\n"
      ".Lk_wo_5:\n"
      "s_memrealtime s[6:7]\n"
      "s_waitcnt lgkmcnt(0)\n"
      "s_sub_u32 s6, s6, s4\n"
      "s_lshl_b32 s6, s6, 5\n"
      "s_sub_u32 s6, s5, s6\n"
      "s_cmp_lt_i32 s6, 640\n"
      "s_cbranch_scc1 .Lk_pc_6\n"
      "s_sleep 5\n"
      ".Lk_pc_6:\n"
      "s_memrealtime s[6:7]\n"
      "s_waitcnt lgkmcnt(0)\n"
      "s_sub_u32 s6, s6, s4\n"
      "s_lshl_b32 s6, s6, 5\n"
      "s_cmp_lt_i32 s6, s5\n"
      "s_cbranch_scc1 .Lk_pc_6\n"
      "s_cmp_lg_u32 s61, 0\n"
      "s_cbranch_scc1 .Lk_sg_7\n"
      "s_add_u32 m0, s58, 0\n"
      "s_mov_b32 s6, 0\n"
      "buffer_load_dwordx4 v97, s[52:55], s6 offen nt lds\n"
      "s_add_u32 m0, s58, 1024\n"
      "s_mov_b32 s6, 2048\n"
      "buffer_load_dwordx4 v97, s[52:55], s6 offen nt lds\n"
      "s_add_u32 m0, s58, 2048\n"
      "s_mov_b32 s6, 16384\n"
      "buffer_load_dwordx4 v97, s[52:55], s6 offen nt lds\n"
      "s_add_u32 m0, s58, 3072\n"
      "s_mov_b32 s6, 18432\n"
      "buffer_load_dwordx4 v97, s[52:55], s6 offen nt lds\n"
      ".Lk_sg_7:\n"
      "s_cmp_lg_u32 s61, 1\n"
      "s_cbranch_scc1 .Lk_sg_8\n"
      "s_add_u32 m0, s58, 16384\n"
      "s_mov_b32 s6, 0\n"
      "buffer_load_dwordx4 v9, s[48:51], s6 offen nt lds\n"
      "s_add_u32 m0, s58, 17408\n"
      "s_mov_b32 s6, 4096\n"
      "buffer_load_dwordx4 v9, s[48:51], s6 offen nt lds\n"
      ".Lk_sg_8:\n"
      "s_add_u32 s5, s5, 1184\n"
      "s_memrealtime s[6:7]\n"
      "s_waitcnt lgkmcnt(0)\n"
      "s_sub_u32 s6, s6, s4\n"
      "s_lshl_b32 s6, s6, 5\n"
      "s_sub_u32 s6, s5, s6\n"
      "s_cmp_lt_i32 s6, 640\n"
      "s_cbranch_scc1 .Lk_pc_9\n"
      "s_sleep 5\n"
      ".Lk_pc_9:\n"
      "s_memrealtime s[6:7]\n"
      "s_waitcnt lgkmcnt(0)\n"
      "s_sub_u32 s6, s6, s4\n"
      "s_lshl_b32 s6, s6, 5\n"
      "s_cmp_lt_i32 s6, s5\n"
      "s_cbranch_scc1 .Lk_pc_9\n"
      "s_cmp_lg_u32 s61, 0\n"
      "s_cbranch_scc1 .Lk_sg_10\n"
      "s_add_u32 m0, s58, 4096\n"
      "s_mov_b32 s6, 32768\n"
      "buffer_load_dwordx4 v97, s[52:55], s6 offen nt lds\n"
      "s_add_u32 m0, s58, 5120\n"
      "s_mov_b32 s6, 34816\n"
      "buffer_load_dwordx4 v97, s[52:55], s6 offen nt lds\n"
      "s_add_u32 m0, s58, 6144\n"
      "s_mov_b32 s6, 49152\n"
      "buffer_load_dwordx4 v97, s[52:55], s6 offen nt lds\n"
      "s_add_u32 m0, s58, 7168\n"
      "s_mov_b32 s6, 51200\n"
      "buffer_load_dwordx4 v97, s[52:55], s6 offen nt lds\n"
      ".Lk_sg_10:\n"
      "s_cmp_lg_u32 s61, 1\n"
      "s_cbranch_scc1 .Lk_sg_11\n"
      "s_add_u32 m0, s58, 18432\n"
      "s_mov_b32 s6, 8192\n"
      "buffer_load_dwordx4 v9, s[48:51], s6 offen nt lds\n"
      "s_add_u32 m0, s58, 19456\n"
      "s_mov_b32 s6, 12288\n"
      "buffer_load_dwordx4 v9, s[48:51], s6 offen nt lds\n"
      ".Lk_sg_11:\n"
      "s_add_u32 s5, s5, 1184\n"
      "s_memrealtime s[6:7]\n"
      "s_waitcnt lgkmcnt(0)\n"
      "s_sub_u32 s6, s6, s4\n"
      "s_lshl_b32 s6, s6, 5\n"
      "s_sub_u32 s6, s5, s6\n"
      "s_cmp_lt_i32 s6, 640\n"
      "s_cbranch_scc1 .Lk_pc_12\n"
      "s_sleep 5\n"
      ".Lk_pc_12:\n"
      "s_memrealtime s[6:7]\n"
      "s_waitcnt lgkmcnt(0)\n"
      "s_sub_u32 s6, s6, s4\n"
      "s_lshl_b32 s6, s6, 5\n"
      "s_cmp_lt_i32 s6, s5\n"
      "s_cbranch_scc1 .Lk_pc_12\n"
      "s_add_u32 s5, s5, s30\n"
      "s_xor_b32 s80, s83, 0x0\n"
      "buffer_load_dwordx4 v[104:107], v101, s[84:87], s80 offen nt\n"
      "buffer_load_dwordx4 v[108:111], v101, s[84:87], s80 offen offset:128 nt\n"
      "s_memrealtime s[6:7]\n"
      "s_waitcnt lgkmcnt(0)\n"
      "s_sub_u32 s6, s6, s4\n"
      "s_lshl_b32 s6, s6, 5\n"
      "s_sub_u32 s6, s5, s6\n"
      "s_cmp_lt_i32 s6, 640\n"
      "s_cbranch_scc1 .Lk_pc_13\n"
      "s_sleep 5\n"
      ".Lk_pc_13:\n"
      "s_memrealtime s[6:7]\n"
      "s_waitcnt lgkmcnt(0)\n"
      "s_sub_u32 s6, s6, s4\n"
      "s_lshl_b32 s6, s6, 5\n"
      "s_cmp_lt_i32 s6, s5\n"
      "s_cbranch_scc1 .Lk_pc_13\n"
      "s_add_u32 s5, s5, s30\n"
      "s_xor_b32 s80, s83, 0x800\n"
      "buffer_load_dwordx4 v[112:115], v101, s[84:87], s80 offen nt\n"
      "buffer_load_dwordx4 v[116:119], v101, s[84:87], s80 offen offset:128 nt\n"
      "v_bfe_u32 v11, v0, 5, 1\n"
      "v_lshlrev_b32 v96, 11, v11\n"
      "v_bfe_u32 v11, v0, 4, 1\n"
      "v_lshl_add_u32 v96, v11, 8, v96\n"
      "v_bfe_u32 v11, v0, 0, 4\n"
      "v_lshl_add_u32 v96, v11, 3, v96\n"
      "s_and_b32 s68, s57, 0x2\n"
      "s_lshl_b32 s68, s68, 10\n"
      "v_xor_b32 v96, s68, v96\n"
      "s_mov_b32 s64, 0xaaaaaaaa\n"
      "s_mov_b32 s65, 0xaaaaaaaa\n"
      "s_mov_b32 s72, 0xffff0000\n"
      "s_mov_b32 s73, 0xffff0000\n"
      "s_mov_b32 s8, 0\n"
      "s_mov_b32 s9, -1\n"
      "s_mov_b32 s66, 0x10000\n"
      "s_mov_b32 s67, -2\n"
      "s_mov_b32 s62, 0\n"
      "s_getreg_b32 s13, hwreg(HW_REG_XCC_ID, 2, 1)\n"
      "s_and_b32 s59, s57, 0x3\n"
      "s_lshl_b32 s59, s59, 2\n"
      "s_add_u32 s59, s59, 0x27c00\n"
      "v_and_b32 v11, 3, v0\n"
      "v_mul_u32_u24 v11, 0x5555, v11\n"
      "v_and_b32 v1, 1, v0\n"
      "v_mul_u32_u24 v1, 0xaaaa, v1\n"
      "v_add_u32 v1, 0x4444, v1\n"
      "v_lshl_add_u32 v1, v11, 16, v1\n"
      "s_memrealtime s[6:7]\n"
      "s_waitcnt lgkmcnt(0)\n"
      "s_sub_u32 s6, s6, s4\n"
      "s_lshl_b32 s6, s6, 5\n"
      "s_sub_u32 s6, s5, s6\n"
      "s_cmp_lt_i32 s6, 640\n"
      "s_cbranch_scc1 .Lk_pc_14\n"
      "s_sleep 5\n"
      ".Lk_pc_14:\n"
      "s_memrealtime s[6:7]\n"
      "s_waitcnt lgkmcnt(0)\n"
      "s_sub_u32 s6, s6, s4\n"
      "s_lshl_b32 s6, s6, 5\n"
      "s_cmp_lt_i32 s6, s5\n"
      "s_cbranch_scc1 .Lk_pc_14\n"
      "s_add_u32 s5, s5, s30\n"
      "s_xor_b32 s80, s83, 0x2000\n"
      "s_add_u32 m0, s82, 6144\n"
      "s_nop 0\n"
      "buffer_load_dwordx4 v101, s[84:87], s80 offen nt lds\n"
      "s_add_u32 m0, s82, 7040\n"
      "s_nop 0\n"
      "buffer_load_dwordx4 v101, s[84:87], s80 offen offset:128 nt lds\n"
      "s_memrealtime s[6:7]\n"
      "s_waitcnt lgkmcnt(0)\n"
      "s_sub_u32 s6, s6, s4\n"
      "s_lshl_b32 s6, s6, 5\n"
      "s_sub_u32 s6, s5, s6\n"
      "s_cmp_lt_i32 s6, 640\n"
      "s_cbranch_scc1 .Lk_pc_15\n"
      "s_sleep 5\n"
      ".Lk_pc_15:\n"
      "s_memrealtime s[6:7]\n"
      "s_waitcnt lgkmcnt(0)\n"
      "s_sub_u32 s6, s6, s4\n"
      "s_lshl_b32 s6, s6, 5\n"
      "s_cmp_lt_i32 s6, s5\n"
      "s_cbranch_scc1 .Lk_pc_15\n"
      "s_add_u32 s5, s5, s30\n"
      "s_xor_b32 s80, s83, 0x2800\n"
      "buffer_load_dwordx4 v[120:123], v101, s[84:87], s80 offen nt\n"
      "buffer_load_dwordx4 v[124:127], v101, s[84:87], s80 offen offset:128 nt\n"
      "s_memrealtime s[6:7]\n"
      "s_waitcnt lgkmcnt(0)\n"
      "s_sub_u32 s6, s6, s4\n"
      "s_lshl_b32 s6, s6, 5\n"
      "s_sub_u32 s6, s5, s6\n"
      "s_cmp_lt_i32 s6, 640\n"
      "s_cbranch_scc1 .Lk_pc_16\n"
      "s_sleep 5\n"
      ".Lk_pc_16:\n"
      "s_memrealtime s[6:7]\n"
      "s_waitcnt lgkmcnt(0)\n"
      "s_sub_u32 s6, s6, s4\n"
      "s_lshl_b32 s6, s6, 5\n"
      "s_cmp_lt_i32 s6, s5\n"
      "s_cbranch_scc1 .Lk_pc_16\n"
      "s_add_u32 s5, s5, s30\n"
      "s_xor_b32 s80, s83, 0x4000\n"
      "s_add_u32 m0, s82, 0\n"
      "s_nop 0\n"
      "buffer_load_dwordx4 v101, s[84:87], s80 offen nt lds\n"
      "s_add_u32 m0, s82, 896\n"
      "s_nop 0\n"
      "buffer_load_dwordx4 v101, s[84:87], s80 offen offset:128 nt lds\n"
      "s_memrealtime s[6:7]\n"
      "s_waitcnt lgkmcnt(0)\n"
      "s_sub_u32 s6, s6, s4\n"
      "s_lshl_b32 s6, s6, 5\n"
      "s_sub_u32 s6, s5, s6\n"
      "s_cmp_lt_i32 s6, 640\n"
      "s_cbranch_scc1 .Lk_pc_17\n"
      "s_sleep 5\n"
      ".Lk_pc_17:\n"
      "s_memrealtime s[6:7]\n"
      "s_waitcnt lgkmcnt(0)\n"
      "s_sub_u32 s6, s6, s4\n"
      "s_lshl_b32 s6, s6, 5\n"
      "s_cmp_lt_i32 s6, s5\n"
      "s_cbranch_scc1 .Lk_pc_17\n"
      "s_add_u32 s5, s5, s30\n"
      "s_xor_b32 s80, s83, 0x4800\n"
      "buffer_load_dwordx4 v[128:131], v101, s[84:87], s80 offen nt\n"
      "buffer_load_dwordx4 v[132:135], v101, s[84:87], s80 offen offset:128 nt\n"
      "s_waitcnt vmcnt(12)\n"
      "s_barrier\n"
      "s_mov_b32 s68, 106496\n"
      "s_and_b32 s69, s57, 0x3\n"
      "s_lshl_b32 s69, s69, 5\n"
      "v_bfe_u32 v11, v0, 5, 1\n"
      "v_lshl_add_u32 v49, v11, 12, s68\n"
      "v_bfe_u32 v11, v0, 3, 1\n"
      "v_lshl_add_u32 v49, v11, 9, v49\n"
      "v_bfe_u32 v11, v0, 3, 1\n"
      "v_lshl_add_u32 v49, v11, 6, v49\n"
      "v_bfe_u32 v11, v0, 4, 1\n"
      "v_lshl_add_u32 v49, v11, 8, v49\n"
      "v_bfe_u32 v11, v0, 4, 1\n"
      "v_lshl_add_u32 v49, v11, 5, v49\n"
      "v_bfe_u32 v11, v0, 2, 1\n"
      "v_lshl_add_u32 v49, v11, 7, v49\n"
      "v_bfe_u32 v11, v0, 0, 2\n"
      "v_lshl_add_u32 v49, v11, 3, v49\n"
      "v_xor_b32 v49, s69, v49\n"
      "ds_read_b64_tr_b16 v[28:29], v49 offset:0\n"
      "ds_read_b64_tr_b16 v[30:31], v49 offset:1024\n"
      "ds_read_b64_tr_b16 v[32:33], v49 offset:8192\n"
      "ds_read_b64_tr_b16 v[34:35], v49 offset:9216\n"
      "ds_read_b64_tr_b16 v[36:37], v49 offset:2048\n"
      "ds_read_b64_tr_b16 v[38:39], v49 offset:3072\n"
      "ds_read_b64_tr_b16 v[40:41], v49 offset:10240\n"
      "ds_read_b64_tr_b16 v[42:43], v49 offset:11264\n"
      "s_mov_b32 s78, 0xff00ff00\n"
      "s_mov_b32 s79, 0xff00ff00\n"
      "v_mov_b32 v11, 0x3f803f80\n"
      "v_cndmask_b32 v12, v12, v11, s[78:79]\n"
      "v_cndmask_b32 v13, v13, v11, s[78:79]\n"
      "v_cndmask_b32 v14, v14, v11, s[78:79]\n"
      "v_cndmask_b32 v15, v15, v11, s[78:79]\n"
      "v_mov_b64 v[16:17], 0\n"
      "v_mov_b64 v[18:19], 0\n"
      "v_mov_b64 v[20:21], 0\n"
      "v_mov_b64 v[22:23], 0\n"
      "s_waitcnt lgkmcnt(0)\n"
      "v_smfmac_f32_16x16x64_bf16 v[16:19], v[12:15], v[28:35], v1\n"
      "v_smfmac_f32_16x16x64_bf16 v[20:23], v[12:15], v[36:43], v1\n"
      "v_readfirstlane_b32 s70, v44\n"
      "v_readfirstlane_b32 s71, v45\n"
      "s_add_u32 s70, s70, 1\n"
      "s_add_u32 s71, s71, 1\n"
      "s_and_b32 s68, s57, 0x3\n"
      "s_lshl_b32 s68, s68, 6\n"
      "s_add_u32 s68, s68, 0x26000\n"
      "v_bfe_u32 v11, v0, 4, 2\n"
      "v_lshl_add_u32 v48, v11, 8, s68\n"
      "v_bfe_u32 v11, v0, 0, 4\n"
      "v_lshl_add_u32 v48, v11, 2, v48\n"
      "s_and_b32 s68, s57, 0x80\n"
      "s_lshr_b32 s68, s68, 4\n"
      "v_xor_b32 v48, s68, v48\n"
      "v_add_f32 v16, v16, v17\n"
      "v_add_f32 v22, v22, v23\n"
      "v_add_f32 v16, v16, v22\n"
      "ds_write_b32 v48, v16\n"
      "s_memrealtime s[6:7]\n"
      "s_waitcnt lgkmcnt(0)\n"
      "s_sub_u32 s6, s6, s4\n"
      "s_lshl_b32 s6, s6, 5\n"
      "s_sub_u32 s6, s5, s6\n"
      "s_cmp_lt_i32 s6, 640\n"
      "s_cbranch_scc1 .Lk_pc_18\n"
      "s_sleep 5\n"
      ".Lk_pc_18:\n"
      "s_memrealtime s[6:7]\n"
      "s_waitcnt lgkmcnt(0)\n"
      "s_sub_u32 s6, s6, s4\n"
      "s_lshl_b32 s6, s6, 5\n"
      "s_cmp_lt_i32 s6, s5\n"
      "s_cbranch_scc1 .Lk_pc_18\n"
      "s_add_u32 s5, s5, s30\n"
      "s_xor_b32 s80, s83, 0x6000\n"
      "buffer_load_dwordx4 v[136:139], v101, s[84:87], s80 offen nt\n"
      "buffer_load_dwordx4 v[140:143], v101, s[84:87], s80 offen offset:128 nt\n"
      "s_barrier\n"
      "s_mov_b32 s68, 155648\n"
      "v_lshl_add_u32 v48, v0, 4, s68\n"
      "ds_read_b128 v[16:19], v48\n"
      "s_and_b32 s68, s57, 0x1c\n"
      "s_lshl_b32 s68, s68, 13\n"
      "s_and_b32 s79, s57, 0x300\n"
      "s_lshl_b32 s79, s79, 5\n"
      "s_add_u32 s68, s68, s79\n"
      "s_and_b32 s79, s57, 0xe0\n"
      "s_lshl_b32 s79, s79, 3\n"
      "s_add_u32 s68, s68, s79\n"
      "s_add_u32 s68, s68, 0x17c000\n"
      "s_cmp_eq_u32 s56, 0\n"
      "s_cselect_b32 s68, s68, 0x40000000\n"
      "v_bfe_u32 v11, v0, 4, 2\n"
      "v_lshl_add_u32 v48, v11, 11, s68\n"
      "v_bfe_u32 v11, v0, 0, 4\n"
      "v_lshl_add_u32 v48, v11, 4, v48\n"
      "s_mov_b32 s78, 0xffffffff\n"
      "s_mov_b32 s79, 0xffff\n"
      "v_mov_b32 v11, 0x40000000\n"
      "v_cndmask_b32 v48, v11, v48, s[78:79]\n"
      "s_waitcnt lgkmcnt(0)\n"
      "buffer_store_dwordx4 v[16:19], v48, s[40:43], 0 offen sc1 nt\n"
      "s_mov_b32 s68, 0x3f803f80\n"
      "v_dot2_f32_bf16 v50, v24, s68, 0\n"
      "v_dot2_f32_bf16 v51, v24, v24, 0\n"
      "v_dot2_f32_bf16 v50, v25, s68, v50\n"
      "v_dot2_f32_bf16 v51, v25, v25, v51\n"
      "v_dot2_f32_bf16 v50, v26, s68, v50\n"
      "v_dot2_f32_bf16 v51, v26, v26, v51\n"
      "v_dot2_f32_bf16 v50, v27, s68, v50\n"
      "v_dot2_f32_bf16 v51, v27, v27, v51\n"
      "s_nop 2\n"
      "v_permlane32_swap_b32 v50, v51\n"
      "v_add_f32 v50, v50, v51\n"
      "s_nop 1\n"
      "v_add_f32_dpp v50, v50, v50 row_ror:8 row_mask:0xf bank_mask:0xf\n"
      "s_nop 1\n"
      "v_add_f32_dpp v50, v50, v50 row_ror:4 row_mask:0xf bank_mask:0xf\n"
      "s_nop 1\n"
      "v_add_f32_dpp v50, v50, v50 row_ror:2 row_mask:0xf bank_mask:0xf\n"
      "s_nop 1\n"
      "v_add_f32_dpp v50, v50, v50 row_ror:1 row_mask:0xf bank_mask:0xf\n"
      "s_nop 1\n"
      "v_add_f32_dpp v50, v50, v50 row_bcast:15 row_mask:0xa bank_mask:0xf\n"
      "s_nop 0\n"
      "v_readlane_b32 s68, v50, 31\n"
      "v_readlane_b32 s69, v50, 63\n"
      "v_mov_b32 v51, 0xbb000000\n"
      "v_mul_f32 v50, s68, v51\n"
      "v_mul_f32 v51, s69, v51\n"
      "v_fma_f32 v51, v50, v50, v51\n"
      "v_sub_f32 v51, 0x3727c5ac, v51\n"
      "v_max_f32 v51, 0x3727c5ac, v51\n"
      "v_rsq_f32 v51, v51\n"
      "s_nop 0\n"
      "v_mul_f32 v10, 0x3e0293ee, v51\n"
      "v_readfirstlane_b32 s10, v50\n"
      "s_memrealtime s[6:7]\n"
      "s_waitcnt lgkmcnt(0)\n"
      "s_sub_u32 s6, s6, s4\n"
      "s_lshl_b32 s6, s6, 5\n"
      "s_sub_u32 s6, s5, s6\n"
      "s_cmp_lt_i32 s6, 640\n"
      "s_cbranch_scc1 .Lk_pc_19\n"
      "s_sleep 5\n"
      ".Lk_pc_19:\n"
      "s_memrealtime s[6:7]\n"
      "s_waitcnt lgkmcnt(0)\n"
      "s_sub_u32 s6, s6, s4\n"
      "s_lshl_b32 s6, s6, 5\n"
      "s_cmp_lt_i32 s6, s5\n"
      "s_cbranch_scc1 .Lk_pc_19\n"
      "s_add_u32 s5, s5, s30\n"
      "s_xor_b32 s80, s83, 0x6800\n"
      "s_add_u32 m0, s82, 10240\n"
      "s_nop 0\n"
      "buffer_load_dwordx4 v101, s[84:87], s80 offen nt lds\n"
      "s_add_u32 m0, s82, 11136\n"
      "s_nop 0\n"
      "buffer_load_dwordx4 v101, s[84:87], s80 offen offset:128 nt lds\n"
      "s_and_b32 s68, s57, 0x1c\n"
      "s_lshl_b32 s68, s68, 12\n"
      "s_and_b32 s79, s57, 0x3c0\n"
      "s_lshl_b32 s79, s79, 3\n"
      "s_add_u32 s68, s68, s79\n"
      "s_and_b32 s79, s57, 0x20\n"
      "s_lshl_b32 s79, s79, 1\n"
      "s_add_u32 s68, s68, s79\n"
      "s_add_u32 s68, s68, 0x68000\n"
      "v_bfe_u32 v11, v0, 4, 2\n"
      "v_lshl_add_u32 v98, v11, 7, s68\n"
      "v_bfe_u32 v11, v0, 0, 4\n"
      "v_lshl_add_u32 v98, v11, 2, v98\n"
      "s_and_b32 s68, s57, 0x18\n"
      "s_lshl_b32 s68, s68, 11\n"
      "s_and_b32 s79, s57, 0x4\n"
      "s_lshl_b32 s79, s79, 10\n"
      "s_add_u32 s68, s68, s79\n"
      "s_and_b32 s79, s57, 0x3e0\n"
      "s_lshl_b32 s79, s79, 2\n"
      "s_add_u32 s68, s68, s79\n"
      "s_cmp_eq_u32 s56, 2\n"
      "s_cselect_b32 s68, s68, 0x40000000\n"
      "v_bfe_u32 v11, v0, 0, 3\n"
      "v_lshl_add_u32 v53, v11, 4, s68\n"
      "s_mov_b32 s78, 0xff\n"
      "s_mov_b32 s79, 0x0\n"
      "v_mov_b32 v11, 0x40000000\n"
      "v_cndmask_b32 v53, v11, v53, s[78:79]\n"
      "s_cmp_eq_u32 s56, 0\n"
      "s_cselect_b64 s[78:79], -1, 0\n"
      "v_cndmask_b32 v98, v53, v98, s[78:79]\n"
      "s_and_b32 s68, s57, 0x3\n"
      "s_lshl_b32 s68, s68, 9\n"
      "s_add_u32 s68, s68, 0x26000\n"
      "v_bfe_u32 v11, v0, 5, 1\n"
      "v_lshl_add_u32 v99, v11, 8, s68\n"
      "v_bfe_u32 v11, v0, 0, 4\n"
      "v_lshl_add_u32 v99, v11, 4, v99\n"
      "v_bfe_u32 v11, v0, 4, 1\n"
      "v_lshl_add_u32 v99, v11, 3, v99\n"
      "s_mov_b32 s68, 155648\n"
      "v_lshl_add_u32 v100, v0, 3, s68\n"
      "s_memrealtime s[6:7]\n"
      "s_waitcnt lgkmcnt(0)\n"
      "s_sub_u32 s6, s6, s4\n"
      "s_lshl_b32 s6, s6, 5\n"
      "s_sub_u32 s6, s5, s6\n"
      "s_cmp_lt_i32 s6, 640\n"
      "s_cbranch_scc1 .Lk_pc_20\n"
      "s_sleep 5\n"
      ".Lk_pc_20:\n"
      "s_memrealtime s[6:7]\n"
      "s_waitcnt lgkmcnt(0)\n"
      "s_sub_u32 s6, s6, s4\n"
      "s_lshl_b32 s6, s6, 5\n"
      "s_cmp_lt_i32 s6, s5\n"
      "s_cbranch_scc1 .Lk_pc_20\n"
      "s_add_u32 s5, s5, s30\n"
      "s_xor_b32 s80, s83, 0x20000\n"
      "buffer_load_dwordx4 v[144:147], v101, s[84:87], s80 offen nt\n"
      "buffer_load_dwordx4 v[148:151], v101, s[84:87], s80 offen offset:128 nt\n"
      "s_memrealtime s[6:7]\n"
      "s_waitcnt lgkmcnt(0)\n"
      "s_sub_u32 s6, s6, s4\n"
      "s_lshl_b32 s6, s6, 5\n"
      "s_sub_u32 s6, s5, s6\n"
      "s_cmp_lt_i32 s6, 640\n"
      "s_cbranch_scc1 .Lk_pc_21\n"
      "s_sleep 5\n"
      ".Lk_pc_21:\n"
      "s_memrealtime s[6:7]\n"
      "s_waitcnt lgkmcnt(0)\n"
      "s_sub_u32 s6, s6, s4\n"
      "s_lshl_b32 s6, s6, 5\n"
      "s_cmp_lt_i32 s6, s5\n"
      "s_cbranch_scc1 .Lk_pc_21\n"
      "s_add_u32 s5, s5, s30\n"
      "s_xor_b32 s80, s83, 0x20800\n"
      "s_add_u32 m0, s82, 4096\n"
      "s_nop 0\n"
      "buffer_load_dwordx4 v101, s[84:87], s80 offen nt lds\n"
      "s_add_u32 m0, s82, 4992\n"
      "s_nop 0\n"
      "buffer_load_dwordx4 v101, s[84:87], s80 offen offset:128 nt lds\n"
      "s_add_u32 s5, s5, 448\n"
      "s_mov_b32 s77, 2\n"
      "s_and_b32 s68, s57, 0x383\n"
      "s_cmp_lg_u32 s68, 0x380\n"
      "s_cbranch_scc1 .Lk_pr_22\n"
      "s_and_b32 s76, s57, 0x20\n"
      "s_lshr_b32 s76, s76, 5\n"
      "s_and_b32 s68, s57, 0x20\n"
      "s_lshl_b32 s68, s68, 8\n"
      "s_and_b32 s79, s57, 0x7c\n"
      "s_lshl_b32 s79, s79, 5\n"
      "s_add_u32 s68, s68, s79\n"
      "s_add_u32 s68, s68, 0x1d8000\n"
      "s_xor_b32 s69, s68, 0x2000\n"
      "s_mov_b64 s[78:79], -1\n"
      "s_buffer_load_dword s78, s[40:43], s68\n"
      "s_buffer_load_dword s79, s[40:43], s69\n"
      "s_waitcnt lgkmcnt(1)\n"
      "s_mov_b64 s[68:69], s[78:79]\n"
      "s_cmp_eq_u32 s68, -1\n"
      "s_cselect_b32 s77, 1, 0\n"
      "s_xor_b32 s77, s77, s76\n"
      "s_xor_b32 s77, s77, s13\n"
      "s_max_u32 s68, s68, s69\n"
      "s_cmp_eq_u32 s68, -1\n"
      "s_cselect_b32 s77, s77, 2\n"
      ".Lk_pr_22:\n"
      "s_lshl2_add_u32 s77, s13, s77\n"
      "v_mov_b32 v47, s77\n"
      "s_memrealtime s[6:7]\n"
      "s_waitcnt lgkmcnt(0)\n"
      "s_sub_u32 s6, s6, s4\n"
      "s_lshl_b32 s6, s6, 5\n"
      "s_sub_u32 s6, s5, s6\n"
      "s_cmp_lt_i32 s6, 640\n"
      "s_cbranch_scc1 .Lk_pc_23\n"
      "s_sleep 5\n"
      ".Lk_pc_23:\n"
      "s_memrealtime s[6:7]\n"
      "s_waitcnt lgkmcnt(0)\n"
      "s_sub_u32 s6, s6, s4\n"
      "s_lshl_b32 s6, s6, 5\n"
      "s_cmp_lt_i32 s6, s5\n"
      "s_cbranch_scc1 .Lk_pc_23\n"
      "s_add_u32 s5, s5, s30\n"
      "s_xor_b32 s80, s83, 0x22000\n"
      "buffer_load_dwordx4 v[152:155], v101, s[84:87], s80 offen nt\n"
      "buffer_load_dwordx4 v[156:159], v101, s[84:87], s80 offen offset:128 nt\n"
      "s_memrealtime s[6:7]\n"
      "s_waitcnt lgkmcnt(0)\n"
      "s_sub_u32 s6, s6, s4\n"
      "s_lshl_b32 s6, s6, 5\n"
      "s_sub_u32 s6, s5, s6\n"
      "s_cmp_lt_i32 s6, 640\n"
      "s_cbranch_scc1 .Lk_pc_24\n"
      "s_sleep 5\n"
      ".Lk_pc_24:\n"
      "s_memrealtime s[6:7]\n"
      "s_waitcnt lgkmcnt(0)\n"
      "s_sub_u32 s6, s6, s4\n"
      "s_lshl_b32 s6, s6, 5\n"
      "s_cmp_lt_i32 s6, s5\n"
      "s_cbranch_scc1 .Lk_pc_24\n"
      "s_add_u32 s5, s5, s30\n"
      "s_xor_b32 s80, s83, 0x22800\n"
      "buffer_load_dwordx4 v[160:163], v101, s[84:87], s80 offen nt\n"
      "buffer_load_dwordx4 v[164:167], v101, s[84:87], s80 offen offset:128 nt\n"
      "s_waitcnt vmcnt(24)\n"
      "s_waitcnt vmcnt(10)\n"
      "s_and_b32 s68, s57, 0x3fc\n"
      "s_lshl_b32 s68, s68, 2\n"
      "s_add_u32 s68, s68, 0x178000\n"
      "s_cmp_eq_u32 s56, 0\n"
      "s_cselect_b32 s68, s68, 0x40000000\n"
      "v_mov_b32 v53, s68\n"
      "s_mov_b32 s78, 0x1\n"
      "s_mov_b32 s79, 0x0\n"
      "v_mov_b32 v11, 0x40000000\n"
      "v_cndmask_b32 v53, v11, v53, s[78:79]\n"
      "v_mov_b32 v44, s70\n"
      "v_mov_b32 v46, s71\n"
      "v_mov_b32 v45, s4\n"
      "s_barrier\n"
      "buffer_store_dwordx4 v[44:47], v53, s[40:43], 0 offen sc1\n"
      "s_memrealtime s[6:7]\n"
      "s_waitcnt lgkmcnt(0)\n"
      "s_sub_u32 s6, s6, s4\n"
      "s_lshl_b32 s6, s6, 5\n"
      "s_sub_u32 s6, s5, s6\n"
      "s_cmp_lt_i32 s6, 640\n"
      "s_cbranch_scc1 .Lk_pc_25\n"
      "s_sleep 5\n"
      ".Lk_pc_25:\n"
      "s_memrealtime s[6:7]\n"
      "s_waitcnt lgkmcnt(0)\n"
      "s_sub_u32 s6, s6, s4\n"
      "s_lshl_b32 s6, s6, 5\n"
      "s_cmp_lt_i32 s6, s5\n"
      "s_cbranch_scc1 .Lk_pc_25\n"
      "s_add_u32 s5, s5, s30\n"
      "s_xor_b32 s80, s83, 0x24000\n"
      "s_add_u32 m0, s82, 2048\n"
      "s_nop 0\n"
      "buffer_load_dwordx4 v101, s[84:87], s80 offen nt lds\n"
      "s_add_u32 m0, s82, 2944\n"
      "s_nop 0\n"
      "buffer_load_dwordx4 v101, s[84:87], s80 offen offset:128 nt lds\n"
      "s_cmp_lg_u32 s61, 0\n"
      "s_cbranch_scc1 .Lk_s2a_26\n"
      "s_and_b32 s68, s57, 0x18\n"
      "s_lshl_b32 s68, s68, 11\n"
      "s_and_b32 s79, s57, 0x4\n"
      "s_lshl_b32 s79, s79, 10\n"
      "s_add_u32 s68, s68, s79\n"
      "s_and_b32 s79, s57, 0x100\n"
      "s_lshl_b32 s79, s79, 3\n"
      "s_add_u32 s68, s68, s79\n"
      "s_and_b32 s79, s57, 0x2\n"
      "s_lshl_b32 s79, s79, 9\n"
      "s_add_u32 s68, s68, s79\n"
      "v_bfe_u32 v11, v0, 5, 1\n"
      "v_lshl_add_u32 v3, v11, 9, s68\n"
      "v_bfe_u32 v11, v0, 2, 2\n"
      "v_lshl_add_u32 v3, v11, 7, v3\n"
      "s_and_b32 s68, s57, 0x1c\n"
      "s_lshl_b32 s68, s68, 12\n"
      "s_and_b32 s79, s57, 0x100\n"
      "s_lshl_b32 s79, s79, 4\n"
      "s_add_u32 s68, s68, s79\n"
      "s_and_b32 s79, s57, 0x2\n"
      "s_lshl_b32 s79, s79, 10\n"
      "s_add_u32 s68, s68, s79\n"
      "s_add_u32 s68, s68, 0x68000\n"
      "v_bfe_u32 v11, v0, 2, 2\n"
      "v_lshl_add_u32 v8, v11, 9, s68\n"
      "v_bfe_u32 v11, v0, 5, 1\n"
      "v_lshl_add_u32 v8, v11, 7, v8\n"
      "v_bfe_u32 v11, v0, 1, 1\n"
      "v_lshl_add_u32 v8, v11, 6, v8\n"
      "v_bfe_u32 v11, v0, 4, 1\n"
      "v_lshl_add_u32 v8, v11, 5, v8\n"
      "v_bfe_u32 v11, v0, 0, 1\n"
      "v_lshl_add_u32 v8, v11, 4, v8\n"
      "s_and_b32 s68, s57, 0x2\n"
      "s_lshl_b32 s68, s68, 13\n"
      "s_add_u32 s68, s68, 0x1e000\n"
      "s_and_b32 s69, s57, 0x1\n"
      "s_lshl_b32 s69, s69, 6\n"
      "v_bfe_u32 v11, v0, 5, 1\n"
      "v_lshl_add_u32 v9, v11, 11, s68\n"
      "v_bfe_u32 v11, v0, 3, 1\n"
      "v_lshl_add_u32 v9, v11, 9, v9\n"
      "v_bfe_u32 v11, v0, 3, 1\n"
      "v_lshl_add_u32 v9, v11, 6, v9\n"
      "v_bfe_u32 v11, v0, 4, 1\n"
      "v_lshl_add_u32 v9, v11, 8, v9\n"
      "v_bfe_u32 v11, v0, 4, 1\n"
      "v_lshl_add_u32 v9, v11, 5, v9\n"
      "v_bfe_u32 v11, v0, 2, 1\n"
      "v_lshl_add_u32 v9, v11, 7, v9\n"
      "v_bfe_u32 v11, v0, 0, 2\n"
      "v_lshl_add_u32 v9, v11, 3, v9\n"
      "v_xor_b32 v9, s69, v9\n"
      ".Lk_s2a_26:\n"
      "s_memrealtime s[6:7]\n"
      "s_waitcnt lgkmcnt(0)\n"
      "s_sub_u32 s6, s6, s4\n"
      "s_lshl_b32 s6, s6, 5\n"
      "s_sub_u32 s6, s5, s6\n"
      "s_cmp_lt_i32 s6, 640\n"
      "s_cbranch_scc1 .Lk_pc_27\n"
      "s_sleep 5\n"
      ".Lk_pc_27:\n"
      "s_memrealtime s[6:7]\n"
      "s_waitcnt lgkmcnt(0)\n"
      "s_sub_u32 s6, s6, s4\n"
      "s_lshl_b32 s6, s6, 5\n"
      "s_cmp_lt_i32 s6, s5\n"
      "s_cbranch_scc1 .Lk_pc_27\n"
      "s_add_u32 s5, s5, s30\n"
      "s_xor_b32 s80, s83, 0x24800\n"
      "buffer_load_dwordx4 v[168:171], v101, s[84:87], s80 offen nt\n"
      "buffer_load_dwordx4 v[172:175], v101, s[84:87], s80 offen offset:128 nt\n"
      "s_memrealtime s[6:7]\n"
      "s_waitcnt lgkmcnt(0)\n"
      "s_sub_u32 s6, s6, s4\n"
      "s_lshl_b32 s6, s6, 5\n"
      "s_sub_u32 s6, s5, s6\n"
      "s_cmp_lt_i32 s6, 640\n"
      "s_cbranch_scc1 .Lk_pc_28\n"
      "s_sleep 5\n"
      ".Lk_pc_28:\n"
      "s_memrealtime s[6:7]\n"
      "s_waitcnt lgkmcnt(0)\n"
      "s_sub_u32 s6, s6, s4\n"
      "s_lshl_b32 s6, s6, 5\n"
      "s_cmp_lt_i32 s6, s5\n"
      "s_cbranch_scc1 .Lk_pc_28\n"
      "s_add_u32 s5, s5, s30\n"
      "s_xor_b32 s80, s83, 0x26000\n"
      "s_add_u32 m0, s82, 8192\n"
      "s_nop 0\n"
      "buffer_load_dwordx4 v101, s[84:87], s80 offen nt lds\n"
      "s_add_u32 m0, s82, 9088\n"
      "s_nop 0\n"
      "buffer_load_dwordx4 v101, s[84:87], s80 offen offset:128 nt lds\n"
      "s_memrealtime s[6:7]\n"
      "s_waitcnt lgkmcnt(0)\n"
      "s_sub_u32 s6, s6, s4\n"
      "s_lshl_b32 s6, s6, 5\n"
      "s_sub_u32 s6, s5, s6\n"
      "s_cmp_lt_i32 s6, 640\n"
      "s_cbranch_scc1 .Lk_pc_29\n"
      "s_sleep 5\n"
      ".Lk_pc_29:\n"
      "s_memrealtime s[6:7]\n"
      "s_waitcnt lgkmcnt(0)\n"
      "s_sub_u32 s6, s6, s4\n"
      "s_lshl_b32 s6, s6, 5\n"
      "s_cmp_lt_i32 s6, s5\n"
      "s_cbranch_scc1 .Lk_pc_29\n"
      "s_add_u32 s5, s5, s30\n"
      "s_xor_b32 s80, s83, 0x26800\n"
      "buffer_load_dwordx4 v[176:179], v101, s[84:87], s80 offen nt\n"
      "buffer_load_dwordx4 v[180:183], v101, s[84:87], s80 offen offset:128 nt\n"
      "s_memrealtime s[6:7]\n"
      "s_waitcnt lgkmcnt(0)\n"
      "s_sub_u32 s6, s6, s4\n"
      "s_lshl_b32 s6, s6, 5\n"
      "s_sub_u32 s6, s5, s6\n"
      "s_cmp_lt_i32 s6, 640\n"
      "s_cbranch_scc1 .Lk_pc_30\n"
      "s_sleep 5\n"
      ".Lk_pc_30:\n"
      "s_memrealtime s[6:7]\n"
      "s_waitcnt lgkmcnt(0)\n"
      "s_sub_u32 s6, s6, s4\n"
      "s_lshl_b32 s6, s6, 5\n"
      "s_cmp_lt_i32 s6, s5\n"
      "s_cbranch_scc1 .Lk_pc_30\n"
      "s_add_u32 s5, s5, s30\n"
      "s_xor_b32 s80, s83, 0x1000\n"
      "s_add_u32 m0, s82, 12288\n"
      "s_nop 0\n"
      "buffer_load_dwordx4 v101, s[84:87], s80 offen nt lds\n"
      "s_add_u32 m0, s82, 13184\n"
      "s_nop 0\n"
      "buffer_load_dwordx4 v101, s[84:87], s80 offen offset:128 nt lds\n"
      "s_memrealtime s[6:7]\n"
      "s_waitcnt lgkmcnt(0)\n"
      "s_sub_u32 s6, s6, s4\n"
      "s_lshl_b32 s6, s6, 5\n"
      "s_sub_u32 s6, s5, s6\n"
      "s_cmp_lt_i32 s6, 640\n"
      "s_cbranch_scc1 .Lk_pc_31\n"
      "s_sleep 5\n"
      ".Lk_pc_31:\n"
      "s_memrealtime s[6:7]\n"
      "s_waitcnt lgkmcnt(0)\n"
      "s_sub_u32 s6, s6, s4\n"
      "s_lshl_b32 s6, s6, 5\n"
      "s_cmp_lt_i32 s6, s5\n"
      "s_cbranch_scc1 .Lk_pc_31\n"
      "s_add_u32 s5, s5, s30\n"
      "s_xor_b32 s80, s83, 0x1800\n"
      "buffer_load_dwordx4 v[184:187], v101, s[84:87], s80 offen nt\n"
      "buffer_load_dwordx4 v[188:191], v101, s[84:87], s80 offen offset:128 nt\n"
      "s_memrealtime s[6:7]\n"
      "s_waitcnt lgkmcnt(0)\n"
      "s_sub_u32 s6, s6, s4\n"
      "s_lshl_b32 s6, s6, 5\n"
      "s_sub_u32 s6, s5, s6\n"
      "s_cmp_lt_i32 s6, 640\n"
      "s_cbranch_scc1 .Lk_pc_32\n"
      "s_sleep 5\n"
      ".Lk_pc_32:\n"
      "s_memrealtime s[6:7]\n"
      "s_waitcnt lgkmcnt(0)\n"
      "s_sub_u32 s6, s6, s4\n"
      "s_lshl_b32 s6, s6, 5\n"
      "s_cmp_lt_i32 s6, s5\n"
      "s_cbranch_scc1 .Lk_pc_32\n"
      "s_add_u32 s5, s5, s30\n"
      "s_xor_b32 s80, s83, 0x3000\n"
      "s_add_u32 m0, s82, 14336\n"
      "s_nop 0\n"
      "buffer_load_dwordx4 v101, s[84:87], s80 offen nt lds\n"
      "s_add_u32 m0, s82, 15232\n"
      "s_nop 0\n"
      "buffer_load_dwordx4 v101, s[84:87], s80 offen offset:128 nt lds\n"
      "s_waitcnt vmcnt(24)\n"
      "s_memrealtime s[6:7]\n"
      "s_waitcnt lgkmcnt(0)\n"
      "s_sub_u32 s6, s6, s4\n"
      "s_lshl_b32 s6, s6, 5\n"
      "s_sub_u32 s6, s5, s6\n"
      "s_cmp_lt_i32 s6, 640\n"
      "s_cbranch_scc1 .Lk_pc_33\n"
      "s_sleep 5\n"
      ".Lk_pc_33:\n"
      "s_memrealtime s[6:7]\n"
      "s_waitcnt lgkmcnt(0)\n"
      "s_sub_u32 s6, s6, s4\n"
      "s_lshl_b32 s6, s6, 5\n"
      "s_cmp_lt_i32 s6, s5\n"
      "s_cbranch_scc1 .Lk_pc_33\n"
      "s_add_u32 s5, s5, s30\n"
      "s_xor_b32 s80, s83, 0x3800\n"
      "buffer_load_dwordx4 v[192:195], v101, s[84:87], s80 offen nt\n"
      "buffer_load_dwordx4 v[196:199], v101, s[84:87], s80 offen offset:128 nt\n"
      "s_waitcnt vmcnt(24)\n"
      "s_memrealtime s[6:7]\n"
      "s_waitcnt lgkmcnt(0)\n"
      "s_sub_u32 s6, s6, s4\n"
      "s_lshl_b32 s6, s6, 5\n"
      "s_sub_u32 s6, s5, s6\n"
      "s_cmp_lt_i32 s6, 640\n"
      "s_cbranch_scc1 .Lk_pc_34\n"
      "s_sleep 5\n"
      ".Lk_pc_34:\n"
      "s_memrealtime s[6:7]\n"
      "s_waitcnt lgkmcnt(0)\n"
      "s_sub_u32 s6, s6, s4\n"
      "s_lshl_b32 s6, s6, 5\n"
      "s_cmp_lt_i32 s6, s5\n"
      "s_cbranch_scc1 .Lk_pc_34\n"
      "s_add_u32 s5, s5, s30\n"
      "s_xor_b32 s80, s83, 0x5000\n"
      "buffer_load_dwordx4 v[200:203], v101, s[84:87], s80 offen nt\n"
      "buffer_load_dwordx4 v[204:207], v101, s[84:87], s80 offen offset:128 nt\n"
      "s_waitcnt vmcnt(24)\n"
      "s_memrealtime s[6:7]\n"
      "s_waitcnt lgkmcnt(0)\n"
      "s_sub_u32 s6, s6, s4\n"
      "s_lshl_b32 s6, s6, 5\n"
      "s_sub_u32 s6, s5, s6\n"
      "s_cmp_lt_i32 s6, 640\n"
      "s_cbranch_scc1 .Lk_pc_35\n"
      "s_sleep 5\n"
      ".Lk_pc_35:\n"
      "s_memrealtime s[6:7]\n"
      "s_waitcnt lgkmcnt(0)\n"
      "s_sub_u32 s6, s6, s4\n"
      "s_lshl_b32 s6, s6, 5\n"
      "s_cmp_lt_i32 s6, s5\n"
      "s_cbranch_scc1 .Lk_pc_35\n"
      "s_add_u32 s5, s5, s30\n"
      "s_xor_b32 s80, s83, 0x5800\n"
      "s_add_u32 m0, s82, 20480\n"
      "s_nop 0\n"
      "buffer_load_dwordx4 v101, s[84:87], s80 offen nt lds\n"
      "s_add_u32 m0, s82, 21376\n"
      "s_nop 0\n"
      "buffer_load_dwordx4 v101, s[84:87], s80 offen offset:128 nt lds\n"
      "s_waitcnt vmcnt(24)\n"
      "s_and_b32 s68, s57, 0x3\n"
      "s_lshl_b32 s68, s68, 10\n"
      "s_add_u32 s68, s68, 0x178000\n"
      "v_lshl_add_u32 v53, v0, 4, s68\n"
      "buffer_load_dwordx4 v[44:47], v53, s[40:43], 0 offen sc1\n"
      "s_memrealtime s[6:7]\n"
      "s_waitcnt lgkmcnt(0)\n"
      "s_sub_u32 s6, s6, s4\n"
      "s_lshl_b32 s6, s6, 5\n"
      "s_sub_u32 s6, s5, s6\n"
      "s_cmp_lt_i32 s6, 640\n"
      "s_cbranch_scc1 .Lk_pc_36\n"
      "s_sleep 5\n"
      ".Lk_pc_36:\n"
      "s_memrealtime s[6:7]\n"
      "s_waitcnt lgkmcnt(0)\n"
      "s_sub_u32 s6, s6, s4\n"
      "s_lshl_b32 s6, s6, 5\n"
      "s_cmp_lt_i32 s6, s5\n"
      "s_cbranch_scc1 .Lk_pc_36\n"
      "s_add_u32 s5, s5, s30\n"
      "s_xor_b32 s80, s83, 0x7000\n"
      "buffer_load_dwordx4 v[208:211], v101, s[84:87], s80 offen nt\n"
      "buffer_load_dwordx4 v[212:215], v101, s[84:87], s80 offen offset:128 nt\n"
      "s_waitcnt vmcnt(24)\n"
      "s_memrealtime s[6:7]\n"
      "s_waitcnt lgkmcnt(0)\n"
      "s_sub_u32 s6, s6, s4\n"
      "s_lshl_b32 s6, s6, 5\n"
      "s_sub_u32 s6, s5, s6\n"
      "s_cmp_lt_i32 s6, 640\n"
      "s_cbranch_scc1 .Lk_pc_37\n"
      "s_sleep 5\n"
      ".Lk_pc_37:\n"
      "s_memrealtime s[6:7]\n"
      "s_waitcnt lgkmcnt(0)\n"
      "s_sub_u32 s6, s6, s4\n"
      "s_lshl_b32 s6, s6, 5\n"
      "s_cmp_lt_i32 s6, s5\n"
      "s_cbranch_scc1 .Lk_pc_37\n"
      "s_add_u32 s5, s5, s30\n"
      "s_xor_b32 s80, s83, 0x7800\n"
      "s_add_u32 m0, s82, 22528\n"
      "s_nop 0\n"
      "buffer_load_dwordx4 v101, s[84:87], s80 offen nt lds\n"
      "s_add_u32 m0, s82, 23424\n"
      "s_nop 0\n"
      "buffer_load_dwordx4 v101, s[84:87], s80 offen offset:128 nt lds\n"
      "s_waitcnt vmcnt(24)\n"
      "s_memrealtime s[6:7]\n"
      "s_waitcnt lgkmcnt(0)\n"
      "s_sub_u32 s6, s6, s4\n"
      "s_lshl_b32 s6, s6, 5\n"
      "s_sub_u32 s6, s5, s6\n"
      "s_cmp_lt_i32 s6, 640\n"
      "s_cbranch_scc1 .Lk_pc_38\n"
      "s_sleep 5\n"
      ".Lk_pc_38:\n"
      "s_memrealtime s[6:7]\n"
      "s_waitcnt lgkmcnt(0)\n"
      "s_sub_u32 s6, s6, s4\n"
      "s_lshl_b32 s6, s6, 5\n"
      "s_cmp_lt_i32 s6, s5\n"
      "s_cbranch_scc1 .Lk_pc_38\n"
      "s_add_u32 s5, s5, s30\n"
      "s_xor_b32 s80, s83, 0x21000\n"
      "buffer_load_dwordx4 v[216:219], v101, s[84:87], s80 offen nt\n"
      "buffer_load_dwordx4 v[220:223], v101, s[84:87], s80 offen offset:128 nt\n"
      "s_waitcnt vmcnt(24)\n"
      "s_memrealtime s[6:7]\n"
      "s_waitcnt lgkmcnt(0)\n"
      "s_sub_u32 s6, s6, s4\n"
      "s_lshl_b32 s6, s6, 5\n"
      "s_sub_u32 s6, s5, s6\n"
      "s_cmp_lt_i32 s6, 640\n"
      "s_cbranch_scc1 .Lk_pc_39\n"
      "s_sleep 5\n"
      ".Lk_pc_39:\n"
      "s_memrealtime s[6:7]\n"
      "s_waitcnt lgkmcnt(0)\n"
      "s_sub_u32 s6, s6, s4\n"
      "s_lshl_b32 s6, s6, 5\n"
      "s_cmp_lt_i32 s6, s5\n"
      "s_cbranch_scc1 .Lk_pc_39\n"
      "s_add_u32 s5, s5, s30\n"
      "s_xor_b32 s80, s83, 0x21800\n"
      "s_add_u32 m0, s82, 16384\n"
      "s_nop 0\n"
      "buffer_load_dwordx4 v101, s[84:87], s80 offen nt lds\n"
      "s_add_u32 m0, s82, 17280\n"
      "s_nop 0\n"
      "buffer_load_dwordx4 v101, s[84:87], s80 offen offset:128 nt lds\n"
      "s_waitcnt vmcnt(24)\n"
      "s_waitcnt vmcnt(8)\n"
      ".Lk_fchk:\n"
      "v_xor_b32 v44, s70, v44\n"
      "v_bitop3_b32 v44, v46, s71, v44 bitop3:0xbe\n"
      "v_cmp_ne_u32 vcc, 0, v44\n"
      "s_cbranch_vccz .Lk_fok\n"
      "buffer_load_dwordx4 v[44:47], v53, s[40:43], 0 offen sc1\n"
      "s_sub_u32 s66, s66, 1\n"
      "s_cselect_b32 s67, -1, s67\n"
      "s_waitcnt vmcnt(0)\n"
      "s_memrealtime s[6:7]\n"
      "s_waitcnt lgkmcnt(0)\n"
      "s_sub_u32 s6, s6, s4\n"
      "s_lshl_b32 s6, s6, 5\n"
      "s_max_i32 s5, s5, s6\n"
      "s_cmp_eq_u32 s67, -1\n"
      "s_cbranch_scc0 .Lk_fchk\n"
      ".Lk_fok:\n"
      "v_and_b32 v46, 3, v47\n"
      "v_cmp_lt_u32 vcc, 3, v47\n"
      "v_cmp_eq_u32 s[68:69], 0, v46\n"
      "v_cmp_eq_u32 s[76:77], 1, v46\n"
      "v_readfirstlane_b32 s7, v45\n"
      "s_bcnt1_i32_b64 s68, s[68:69]\n"
      "s_bcnt1_i32_b64 s76, s[76:77]\n"
      "s_sub_u32 s68, s76, s68\n"
      "s_and_b32 s69, vcc_lo, 0xff\n"
      "s_lshl_b32 s69, s69, 16\n"
      "s_cmp_eq_u32 s56, 0\n"
      "s_cselect_b32 s69, s69, 0\n"
      "s_add_u32 s68, s68, s69\n"
      "v_mov_b32 v46, s68\n"
      "v_subrev_u32 v47, s7, v45\n"
      "s_nop 1\n"
      "v_add_u32_dpp v47, v47, v47 row_ror:8 row_mask:0xf bank_mask:0xf\n"
      "s_nop 1\n"
      "v_add_u32_dpp v47, v47, v47 row_ror:4 row_mask:0xf bank_mask:0xf\n"
      "s_nop 1\n"
      "v_add_u32_dpp v47, v47, v47 row_ror:2 row_mask:0xf bank_mask:0xf\n"
      "s_nop 1\n"
      "v_add_u32_dpp v47, v47, v47 row_ror:1 row_mask:0xf bank_mask:0xf\n"
      "s_nop 1\n"
      "v_add_u32_dpp v47, v47, v47 row_bcast:15 row_mask:0xa bank_mask:0xf\n"
      "s_nop 1\n"
      "v_add_u32_dpp v47, v47, v47 row_bcast:31 row_mask:0xc bank_mask:0xf\n"
      "v_ashrrev_i32 v47, 6, v47\n"
      "v_add_u32 v47, s7, v47\n"
      "v_lshrrev_b32 v53, 4, v0\n"
      "v_lshl_add_u32 v53, v53, 5, s59\n"
      "ds_write2_b32 v53, v47, v46 offset0:64 offset1:68\n"
      "v_mov_b32 v53, 163168\n"
      "s_waitcnt lgkmcnt(0)\n"
      "s_barrier\n"
      "ds_read_b128 v[48:51], v53\n"
      "ds_read_b128 v[56:59], v53 offset:16\n"
      "s_mov_b32 s68, 1544192\n"
      "s_cmp_eq_u32 s57, 0\n"
      "s_cselect_b32 s68, s68, 0x40000000\n"
      "v_mov_b32 v53, s68\n"
      "s_mov_b32 s78, 0x1\n"
      "s_mov_b32 s79, 0x0\n"
      "v_mov_b32 v11, 0x40000000\n"
      "v_cndmask_b32 v53, v11, v53, s[78:79]\n"
      "v_mov_b32 v44, s70\n"
      "v_mov_b32 v45, s71\n"
      "buffer_store_dwordx2 v[44:45], v53, s[40:43], 0 offen sc1\n"
      "s_waitcnt lgkmcnt(0)\n"
      "v_subrev_u32 v48, s4, v48\n"
      "v_subrev_u32 v49, s4, v49\n"
      "v_subrev_u32 v50, s4, v50\n"
      "v_subrev_u32 v51, s4, v51\n"
      "v_add3_u32 v48, v48, v49, v50\n"
      "v_add3_u32 v48, v48, v51, 2\n"
      "v_ashrrev_i32 v48, 2, v48\n"
      "v_min_i32 v48, 100, v48\n"
      "v_max_i32 v48, 0xffffff9c, v48\n"
      "v_add3_u32 v56, v56, v57, v58\n"
      "v_add_u32 v56, v56, v59\n"
      "v_readfirstlane_b32 s7, v48\n"
      "v_readfirstlane_b32 s68, v56\n"
      "s_nop 0\n"
      "s_add_u32 s21, s68, 0x8000\n"
      "s_lshr_b32 s21, s21, 16\n"
      "s_sext_i32_i16 s68, s68\n"
      "s_cmp_gt_i32 s68, 0\n"
      "s_cselect_b32 s68, 0xff, 0\n"
      "s_xor_b32 s21, s21, s68\n"
      "s_and_b32 s68, s57, 0x1c\n"
      "s_lshr_b32 s68, s68, 2\n"
      "s_lshr_b32 s63, s21, s68\n"
      "s_and_b32 s63, s63, 1\n"
      "s_lshl_b32 s63, s63, 13\n"
      "s_mul_i32 s7, s7, 221\n"
      "s_ashr_i32 s7, s7, 8\n"
      "s_add_u32 s30, s30, s7\n"
      "s_memrealtime s[6:7]\n"
      "s_waitcnt lgkmcnt(0)\n"
      "s_sub_u32 s6, s6, s4\n"
      "s_lshl_b32 s6, s6, 5\n"
      "s_sub_u32 s6, s5, s6\n"
      "s_cmp_lt_i32 s6, 640\n"
      "s_cbranch_scc1 .Lk_pc_40\n"
      "s_sleep 5\n"
      ".Lk_pc_40:\n"
      "s_memrealtime s[6:7]\n"
      "s_waitcnt lgkmcnt(0)\n"
      "s_sub_u32 s6, s6, s4\n"
      "s_lshl_b32 s6, s6, 5\n"
      "s_cmp_lt_i32 s6, s5\n"
      "s_cbranch_scc1 .Lk_pc_40\n"
      "s_add_u32 s5, s5, s30\n"
      "s_xor_b32 s80, s83, 0x23000\n"
      "buffer_load_dwordx4 v[224:227], v101, s[84:87], s80 offen nt\n"
      "buffer_load_dwordx4 v[228:231], v101, s[84:87], s80 offen offset:128 nt\n"
      "s_and_b32 s69, s57, 0x80\n"
      "s_lshl_b32 s69, s69, 4\n"
      "s_cmp_lt_u32 s56, 2\n"
      "s_cselect_b32 s69, s69, 4096\n"
      "s_and_b32 s68, s57, 0x1c\n"
      "s_lshl_b32 s68, s68, 13\n"
      "s_and_b32 s79, s57, 0x300\n"
      "s_lshl_b32 s79, s79, 5\n"
      "s_add_u32 s68, s68, s79\n"
      "s_and_b32 s79, s57, 0x1\n"
      "s_lshl_b32 s79, s79, 9\n"
      "s_add_u32 s68, s68, s79\n"
      "s_add_u32 s68, s68, 0x17c000\n"
      "v_bfe_u32 v11, v0, 3, 3\n"
      "v_lshl_add_u32 v52, v11, 6, s68\n"
      "v_bfe_u32 v11, v0, 0, 1\n"
      "v_lshl_add_u32 v52, v11, 10, v52\n"
      "v_bfe_u32 v11, v0, 1, 1\n"
      "v_lshl_add_u32 v52, v11, 5, v52\n"
      "v_bfe_u32 v11, v0, 2, 1\n"
      "v_lshl_add_u32 v52, v11, 4, v52\n"
      "buffer_load_dwordx4 v[88:91], v52, s[40:43], s69 offen sc1 nt\n"
      "s_memrealtime s[6:7]\n"
      "s_waitcnt lgkmcnt(0)\n"
      "s_sub_u32 s6, s6, s4\n"
      "s_lshl_b32 s6, s6, 5\n"
      "s_sub_u32 s6, s5, s6\n"
      "s_cmp_lt_i32 s6, 640\n"
      "s_cbranch_scc1 .Lk_pc_41\n"
      "s_sleep 5\n"
      ".Lk_pc_41:\n"
      "s_memrealtime s[6:7]\n"
      "s_waitcnt lgkmcnt(0)\n"
      "s_sub_u32 s6, s6, s4\n"
      "s_lshl_b32 s6, s6, 5\n"
      "s_cmp_lt_i32 s6, s5\n"
      "s_cbranch_scc1 .Lk_pc_41\n"
      "s_add_u32 s5, s5, s30\n"
      "s_xor_b32 s80, s83, 0x23800\n"
      "s_add_u32 m0, s82, 18432\n"
      "s_nop 0\n"
      "buffer_load_dwordx4 v101, s[84:87], s80 offen nt lds\n"
      "s_add_u32 m0, s82, 19328\n"
      "s_nop 0\n"
      "buffer_load_dwordx4 v101, s[84:87], s80 offen offset:128 nt lds\n"
      "s_memrealtime s[6:7]\n"
      "s_waitcnt lgkmcnt(0)\n"
      "s_sub_u32 s6, s6, s4\n"
      "s_lshl_b32 s6, s6, 5\n"
      "s_sub_u32 s6, s5, s6\n"
      "s_cmp_lt_i32 s6, 640\n"
      "s_cbranch_scc1 .Lk_pc_42\n"
      "s_sleep 5\n"
      ".Lk_pc_42:\n"
      "s_memrealtime s[6:7]\n"
      "s_waitcnt lgkmcnt(0)\n"
      "s_sub_u32 s6, s6, s4\n"
      "s_lshl_b32 s6, s6, 5\n"
      "s_cmp_lt_i32 s6, s5\n"
      "s_cbranch_scc1 .Lk_pc_42\n"
      "s_add_u32 s5, s5, s30\n"
      "s_xor_b32 s80, s83, 0x25000\n"
      "buffer_load_dwordx4 v[232:235], v101, s[84:87], s80 offen nt\n"
      "buffer_load_dwordx4 v[236:239], v101, s[84:87], s80 offen offset:128 nt\n"
      "s_add_u32 s5, s5, 448\n"
      "s_memrealtime s[6:7]\n"
      "s_waitcnt lgkmcnt(0)\n"
      "s_sub_u32 s6, s6, s4\n"
      "s_lshl_b32 s6, s6, 5\n"
      "s_sub_u32 s6, s5, s6\n"
      "s_cmp_lt_i32 s6, 640\n"
      "s_cbranch_scc1 .Lk_pc_43\n"
      "s_sleep 5\n"
      ".Lk_pc_43:\n"
      "s_memrealtime s[6:7]\n"
      "s_waitcnt lgkmcnt(0)\n"
      "s_sub_u32 s6, s6, s4\n"
      "s_lshl_b32 s6, s6, 5\n"
      "s_cmp_lt_i32 s6, s5\n"
      "s_cbranch_scc1 .Lk_pc_43\n"
      "s_add_u32 s5, s5, s30\n"
      "s_xor_b32 s80, s83, 0x25800\n"
      "s_add_u32 m0, s82, 24576\n"
      "s_nop 0\n"
      "buffer_load_dwordx4 v101, s[84:87], s80 offen nt lds\n"
      "s_add_u32 m0, s82, 25472\n"
      "s_nop 0\n"
      "buffer_load_dwordx4 v101, s[84:87], s80 offen offset:128 nt lds\n"
      "s_memrealtime s[6:7]\n"
      "s_waitcnt lgkmcnt(0)\n"
      "s_sub_u32 s6, s6, s4\n"
      "s_lshl_b32 s6, s6, 5\n"
      "s_sub_u32 s6, s5, s6\n"
      "s_cmp_lt_i32 s6, 640\n"
      "s_cbranch_scc1 .Lk_pc_44\n"
      "s_sleep 5\n"
      ".Lk_pc_44:\n"
      "s_memrealtime s[6:7]\n"
      "s_waitcnt lgkmcnt(0)\n"
      "s_sub_u32 s6, s6, s4\n"
      "s_lshl_b32 s6, s6, 5\n"
      "s_cmp_lt_i32 s6, s5\n"
      "s_cbranch_scc1 .Lk_pc_44\n"
      "s_add_u32 s5, s5, s30\n"
      "s_xor_b32 s80, s83, 0x27000\n"
      "buffer_load_dwordx4 v[240:243], v101, s[84:87], s80 offen nt\n"
      "buffer_load_dwordx4 v[244:247], v101, s[84:87], s80 offen offset:128 nt\n"
      "s_memrealtime s[6:7]\n"
      "s_waitcnt lgkmcnt(0)\n"
      "s_sub_u32 s6, s6, s4\n"
      "s_lshl_b32 s6, s6, 5\n"
      "s_sub_u32 s6, s5, s6\n"
      "s_cmp_lt_i32 s6, 640\n"
      "s_cbranch_scc1 .Lk_pc_45\n"
      "s_sleep 5\n"
      ".Lk_pc_45:\n"
      "s_memrealtime s[6:7]\n"
      "s_waitcnt lgkmcnt(0)\n"
      "s_sub_u32 s6, s6, s4\n"
      "s_lshl_b32 s6, s6, 5\n"
      "s_cmp_lt_i32 s6, s5\n"
      "s_cbranch_scc1 .Lk_pc_45\n"
      "s_add_u32 s5, s5, s30\n"
      "s_xor_b32 s80, s83, 0x27800\n"
      "buffer_load_dwordx4 v[248:251], v101, s[84:87], s80 offen nt\n"
      "buffer_load_dwordx4 v[252:255], v101, s[84:87], s80 offen offset:128 nt\n"
      "s_and_b32 s68, s57, 0x2\n"
      "s_lshl_b32 s68, s68, 10\n"
      "s_and_b32 s79, s57, 0x2\n"
      "s_lshl_b32 s79, s79, 9\n"
      "s_add_u32 s68, s68, s79\n"
      "s_xor_b32 s83, s83, s68\n"
      "s_memrealtime s[6:7]\n"
      "s_waitcnt lgkmcnt(0)\n"
      "s_sub_u32 s6, s6, s4\n"
      "s_lshl_b32 s6, s6, 5\n"
      "s_sub_u32 s6, s5, s6\n"
      "s_cmp_lt_i32 s6, 640\n"
      "s_cbranch_scc1 .Lk_pc_46\n"
      "s_sleep 5\n"
      ".Lk_pc_46:\n"
      "s_memrealtime s[6:7]\n"
      "s_waitcnt lgkmcnt(0)\n"
      "s_sub_u32 s6, s6, s4\n"
      "s_lshl_b32 s6, s6, 5\n"
      "s_cmp_lt_i32 s6, s5\n"
      "s_cbranch_scc1 .Lk_pc_46\n"
      "s_add_u32 s5, s5, s30\n"
      "s_xor_b32 s81, s83, 0x0\n"
      "buffer_load_dwordx2 a[0:1], v96, s[88:91], s81 offen nt\n"
      "buffer_load_dwordx2 a[8:9], v96, s[88:91], s81 offen offset:128 nt\n"
      "buffer_load_dwordx2 a[16:17], v96, s[88:91], s81 offen offset:512 nt\n"
      "buffer_load_dwordx2 a[24:25], v96, s[88:91], s81 offen offset:640 nt\n"
      "v_mov_b64 v[12:13], 0\n"
      "v_mov_b64 v[14:15], 0\n"
      "v_mov_b64 v[16:17], 0\n"
      "v_mov_b64 v[18:19], 0\n"
      "v_mov_b64 v[20:21], 0\n"
      "v_mov_b64 v[22:23], 0\n"
      "v_mov_b64 v[24:25], 0\n"
      "v_mov_b64 v[26:27], 0\n"
      "v_mov_b64 v[28:29], 0\n"
      "v_mov_b64 v[30:31], 0\n"
      "v_mov_b64 v[32:33], 0\n"
      "v_mov_b64 v[34:35], 0\n"
      "v_mov_b64 v[36:37], 0\n"
      "v_mov_b64 v[38:39], 0\n"
      "v_mov_b64 v[40:41], 0\n"
      "v_mov_b64 v[42:43], 0\n"
      "v_mov_b64 v[44:45], 0\n"
      "v_mov_b64 v[46:47], 0\n"
      "v_mov_b64 v[48:49], 0\n"
      "v_mov_b64 v[50:51], 0\n"
      "v_mov_b64 v[52:53], 0\n"
      "v_mov_b64 v[54:55], 0\n"
      "v_mov_b64 v[56:57], 0\n"
      "v_mov_b64 v[58:59], 0\n"
      "s_and_b32 s68, s57, 0x3\n"
      "s_lshl_b32 s68, s68, 10\n"
      "s_add_u32 s68, s68, 0x26000\n"
      "v_lshl_add_u32 v60, v0, 4, s68\n"
      "s_mov_b32 s68, 155648\n"
      "v_lshl_add_u32 v61, v0, 4, s68\n"
      "s_waitcnt vmcnt(14)\n"
      "ds_write_b128 v60, v[88:91]\n"
      "s_memrealtime s[6:7]\n"
      "s_waitcnt lgkmcnt(0)\n"
      "s_sub_u32 s6, s6, s4\n"
      "s_lshl_b32 s6, s6, 5\n"
      "s_sub_u32 s6, s5, s6\n"
      "s_cmp_lt_i32 s6, 640\n"
      "s_cbranch_scc1 .Lk_pc_47\n"
      "s_sleep 5\n"
      ".Lk_pc_47:\n"
      "s_memrealtime s[6:7]\n"
      "s_waitcnt lgkmcnt(0)\n"
      "s_sub_u32 s6, s6, s4\n"
      "s_lshl_b32 s6, s6, 5\n"
      "s_cmp_lt_i32 s6, s5\n"
      "s_cbranch_scc1 .Lk_pc_47\n"
      "s_add_u32 s5, s5, s30\n"
      "s_xor_b32 s81, s83, 0x400\n"
      "buffer_load_dwordx2 a[32:33], v96, s[88:91], s81 offen nt\n"
      "buffer_load_dwordx2 a[40:41], v96, s[88:91], s81 offen offset:128 nt\n"
      "buffer_load_dwordx2 a[48:49], v96, s[88:91], s81 offen offset:512 nt\n"
      "buffer_load_dwordx2 a[56:57], v96, s[88:91], s81 offen offset:640 nt\n"
      "s_barrier\n"
      "ds_read_b128 v[88:91], v61 offset:0\n"
      "ds_read_b128 v[92:95], v61 offset:1024\n"
      "ds_read_b128 v[4:7], v61 offset:2048\n"
      "ds_read_b128 v[84:87], v61 offset:3072\n"
      "s_waitcnt lgkmcnt(0)\n"
      "v_add_f32 v4, v4, v84\n"
      "v_add_f32 v5, v5, v85\n"
      "v_add_f32 v6, v6, v86\n"
      "v_add_f32 v7, v7, v87\n"
      "v_mul_f32 v84, s10, v4\n"
      "v_mul_f32 v85, s10, v5\n"
      "v_mul_f32 v86, s10, v6\n"
      "v_mul_f32 v87, s10, v7\n"
      "v_add_f32 v84, v84, v88\n"
      "v_add_f32 v85, v85, v89\n"
      "v_add_f32 v86, v86, v90\n"
      "v_add_f32 v87, v87, v91\n"
      "v_pk_add_f32 v[90:91], v[86:87], v[94:95]\n"
      "v_pk_add_f32 v[88:89], v[84:85], v[92:93]\n"
      "s_nop 1\n"
      "v_add_f32_dpp v88, v90, v88 quad_perm:[1,0,3,2] row_mask:0xf bank_mask:0xf\n"
      "v_add_f32_dpp v89, v91, v89 quad_perm:[1,0,3,2] row_mask:0xf bank_mask:0xf\n"
      "v_cvt_pk_bf16_f32 v88, v88, v89\n"
      "s_mov_b32 s74, s63\n"
      "s_mov_b32 s12, s63\n"
      "s_memrealtime s[6:7]\n"
      "s_waitcnt lgkmcnt(0)\n"
      "s_sub_u32 s6, s6, s4\n"
      "s_lshl_b32 s6, s6, 5\n"
      "s_sub_u32 s6, s5, s6\n"
      "s_cmp_lt_i32 s6, 640\n"
      "s_cbranch_scc1 .Lk_pc_48\n"
      "s_sleep 5\n"
      ".Lk_pc_48:\n"
      "s_memrealtime s[6:7]\n"
      "s_waitcnt lgkmcnt(0)\n"
      "s_sub_u32 s6, s6, s4\n"
      "s_lshl_b32 s6, s6, 5\n"
      "s_cmp_lt_i32 s6, s5\n"
      "s_cbranch_scc1 .Lk_pc_48\n"
      "s_add_u32 s5, s5, s30\n"
      "s_xor_b32 s81, s83, 0x2000\n"
      "buffer_load_dwordx2 a[4:5], v96, s[88:91], s81 offen nt\n"
      "buffer_load_dwordx2 a[12:13], v96, s[88:91], s81 offen offset:128 nt\n"
      "buffer_load_dwordx2 a[20:21], v96, s[88:91], s81 offen offset:512 nt\n"
      "buffer_load_dwordx2 a[28:29], v96, s[88:91], s81 offen offset:640 nt\n"
      "s_memrealtime s[6:7]\n"
      "s_waitcnt lgkmcnt(0)\n"
      "s_sub_u32 s6, s6, s4\n"
      "s_lshl_b32 s6, s6, 5\n"
      "s_sub_u32 s6, s5, s6\n"
      "s_cmp_lt_i32 s6, 640\n"
      "s_cbranch_scc1 .Lk_pc_49\n"
      "s_sleep 5\n"
      ".Lk_pc_49:\n"
      "s_memrealtime s[6:7]\n"
      "s_waitcnt lgkmcnt(0)\n"
      "s_sub_u32 s6, s6, s4\n"
      "s_lshl_b32 s6, s6, 5\n"
      "s_cmp_lt_i32 s6, s5\n"
      "s_cbranch_scc1 .Lk_pc_49\n"
      "s_add_u32 s5, s5, s30\n"
      "s_xor_b32 s81, s83, 0x2400\n"
      "buffer_load_dwordx2 a[36:37], v96, s[88:91], s81 offen nt\n"
      "buffer_load_dwordx2 a[44:45], v96, s[88:91], s81 offen offset:128 nt\n"
      "buffer_load_dwordx2 a[52:53], v96, s[88:91], s81 offen offset:512 nt\n"
      "buffer_load_dwordx2 a[60:61], v96, s[88:91], s81 offen offset:640 nt\n"
      "s_memrealtime s[6:7]\n"
      "s_waitcnt lgkmcnt(0)\n"
      "s_sub_u32 s6, s6, s4\n"
      "s_lshl_b32 s6, s6, 5\n"
      "s_sub_u32 s6, s5, s6\n"
      "s_cmp_lt_i32 s6, 640\n"
      "s_cbranch_scc1 .Lk_pc_50\n"
      "s_sleep 5\n"
      ".Lk_pc_50:\n"
      "s_memrealtime s[6:7]\n"
      "s_waitcnt lgkmcnt(0)\n"
      "s_sub_u32 s6, s6, s4\n"
      "s_lshl_b32 s6, s6, 5\n"
      "s_cmp_lt_i32 s6, s5\n"
      "s_cbranch_scc1 .Lk_pc_50\n"
      "s_add_u32 s5, s5, s30\n"
      "s_xor_b32 s81, s83, 0x4000\n"
      "buffer_load_dwordx2 a[2:3], v96, s[88:91], s81 offen nt\n"
      "buffer_load_dwordx2 a[10:11], v96, s[88:91], s81 offen offset:128 nt\n"
      "buffer_load_dwordx2 a[18:19], v96, s[88:91], s81 offen offset:512 nt\n"
      "buffer_load_dwordx2 a[26:27], v96, s[88:91], s81 offen offset:640 nt\n"
      "s_waitcnt vmcnt(20)\n"
      "ds_read_b128 v[60:63], v2 offset:0\n"
      "ds_read_b128 v[64:67], v2 offset:1024\n"
      "ds_read_b128 v[68:71], v2 offset:2048\n"
      "ds_read_b128 v[72:75], v2 offset:3072\n"
      "ds_read_b128 v[76:79], v2 offset:4096\n"
      "ds_read_b128 v[80:83], v2 offset:5120\n"
      "v_mov_b32_dpp v12, v88 quad_perm:[0,1,2,3] row_mask:0xf bank_mask:0x1\n"
      "v_mov_b32_dpp v13, v88 row_ror:12 row_mask:0xf bank_mask:0x1\n"
      "v_mov_b32_dpp v14, v88 row_ror:8 row_mask:0xf bank_mask:0x1\n"
      "v_mov_b32_dpp v15, v88 row_ror:4 row_mask:0xf bank_mask:0x1\n"
      "s_nop 1\n"
      "v_smfmac_f32_16x16x64_bf16 v[28:31], v[12:15], v[104:111], v1\n"
      "s_waitcnt lgkmcnt(4)\n"
      "v_mov_b32_dpp v16, v88 row_ror:4 row_mask:0xf bank_mask:0x2\n"
      "v_mov_b32_dpp v17, v88 quad_perm:[0,1,2,3] row_mask:0xf bank_mask:0x2\n"
      "v_smfmac_f32_16x16x64_bf16 v[32:35], v[12:15], v[60:67], v1\n"
      "ds_read_b128 v[60:63], v2 offset:6144\n"
      "v_mov_b32_dpp v18, v88 row_ror:12 row_mask:0xf bank_mask:0x2\n"
      "v_smfmac_f32_16x16x64_bf16 v[36:39], v[12:15], v[144:151], v1\n"
      "ds_read_b128 v[64:67], v2 offset:7168\n"
      "s_waitcnt lgkmcnt(4)\n"
      "v_mov_b32_dpp v19, v88 row_ror:8 row_mask:0xf bank_mask:0x2\n"
      "v_smfmac_f32_16x16x64_bf16 v[40:43], v[12:15], v[68:75], v1\n"
      "ds_read_b128 v[68:71], v2 offset:8192\n"
      "v_smfmac_f32_16x16x64_bf16 v[28:31], v[16:19], v[112:119], v1\n"
      "ds_read_b128 v[72:75], v2 offset:9216\n"
      "v_mov_b32_dpp v20, v88 row_ror:8 row_mask:0xf bank_mask:0x4\n"
      "v_mov_b32_dpp v21, v88 row_ror:4 row_mask:0xf bank_mask:0x4\n"
      "v_smfmac_f32_16x16x64_bf16 v[32:35], v[16:19], v[128:135], v1\n"
      "s_waitcnt lgkmcnt(4)\n"
      "v_mov_b32_dpp v22, v88 quad_perm:[0,1,2,3] row_mask:0xf bank_mask:0x4\n"
      "v_smfmac_f32_16x16x64_bf16 v[36:39], v[16:19], v[76:83], v1\n"
      "ds_read_b128 v[76:79], v2 offset:10240\n"
      "v_mov_b32_dpp v23, v88 row_ror:12 row_mask:0xf bank_mask:0x4\n"
      "v_smfmac_f32_16x16x64_bf16 v[40:43], v[16:19], v[168:175], v1\n"
      "ds_read_b128 v[80:83], v2 offset:11264\n"
      "s_waitcnt lgkmcnt(4)\n"
      "s_memrealtime s[6:7]\n"
      "s_waitcnt lgkmcnt(0)\n"
      "s_sub_u32 s6, s6, s4\n"
      "s_lshl_b32 s6, s6, 5\n"
      "s_sub_u32 s6, s5, s6\n"
      "s_cmp_lt_i32 s6, 640\n"
      "s_cbranch_scc1 .Lk_pc_51\n"
      "s_sleep 5\n"
      ".Lk_pc_51:\n"
      "s_memrealtime s[6:7]\n"
      "s_waitcnt lgkmcnt(0)\n"
      "s_sub_u32 s6, s6, s4\n"
      "s_lshl_b32 s6, s6, 5\n"
      "s_cmp_lt_i32 s6, s5\n"
      "s_cbranch_scc1 .Lk_pc_51\n"
      "s_add_u32 s5, s5, s30\n"
      "s_xor_b32 s81, s83, 0x4400\n"
      "buffer_load_dwordx2 a[34:35], v96, s[88:91], s81 offen nt\n"
      "buffer_load_dwordx2 a[42:43], v96, s[88:91], s81 offen offset:128 nt\n"
      "buffer_load_dwordx2 a[50:51], v96, s[88:91], s81 offen offset:512 nt\n"
      "buffer_load_dwordx2 a[58:59], v96, s[88:91], s81 offen offset:640 nt\n"
      "v_smfmac_f32_16x16x64_bf16 v[28:31], v[20:23], v[60:67], v1\n"
      "ds_read_b128 v[60:63], v2 offset:12288\n"
      "v_mov_b32_dpp v24, v88 row_ror:12 row_mask:0xf bank_mask:0x8\n"
      "v_mov_b32_dpp v25, v88 row_ror:8 row_mask:0xf bank_mask:0x8\n"
      "v_smfmac_f32_16x16x64_bf16 v[32:35], v[20:23], v[136:143], v1\n"
      "ds_read_b128 v[64:67], v2 offset:13312\n"
      "v_mov_b32_dpp v26, v88 row_ror:4 row_mask:0xf bank_mask:0x8\n"
      "v_smfmac_f32_16x16x64_bf16 v[36:39], v[20:23], v[152:159], v1\n"
      "v_mov_b32_dpp v27, v88 quad_perm:[0,1,2,3] row_mask:0xf bank_mask:0x8\n"
      "v_smfmac_f32_16x16x64_bf16 v[40:43], v[20:23], v[68:75], v1\n"
      "ds_read_b128 v[68:71], v2 offset:14336\n"
      "v_smfmac_f32_16x16x64_bf16 v[28:31], v[24:27], v[120:127], v1\n"
      "ds_read_b128 v[72:75], v2 offset:15360\n"
      "s_nop 0\n"
      "v_smfmac_f32_16x16x64_bf16 v[32:35], v[24:27], v[76:83], v1\n"
      "ds_read_b128 v[76:79], v2 offset:16384\n"
      "s_nop 0\n"
      "v_smfmac_f32_16x16x64_bf16 v[36:39], v[24:27], v[160:167], v1\n"
      "ds_read_b128 v[80:83], v2 offset:17408\n"
      "s_nop 0\n"
      "v_smfmac_f32_16x16x64_bf16 v[40:43], v[24:27], v[176:183], v1\n"
      "s_waitcnt lgkmcnt(4)\n"
      "s_memrealtime s[6:7]\n"
      "s_waitcnt lgkmcnt(0)\n"
      "s_sub_u32 s6, s6, s4\n"
      "s_lshl_b32 s6, s6, 5\n"
      "s_sub_u32 s6, s5, s6\n"
      "s_cmp_lt_i32 s6, 640\n"
      "s_cbranch_scc1 .Lk_pc_52\n"
      "s_sleep 5\n"
      ".Lk_pc_52:\n"
      "s_memrealtime s[6:7]\n"
      "s_waitcnt lgkmcnt(0)\n"
      "s_sub_u32 s6, s6, s4\n"
      "s_lshl_b32 s6, s6, 5\n"
      "s_cmp_lt_i32 s6, s5\n"
      "s_cbranch_scc1 .Lk_pc_52\n"
      "s_add_u32 s5, s5, s30\n"
      "s_xor_b32 s81, s83, 0x6000\n"
      "buffer_load_dwordx2 a[6:7], v96, s[88:91], s81 offen nt\n"
      "buffer_load_dwordx2 a[14:15], v96, s[88:91], s81 offen offset:128 nt\n"
      "buffer_load_dwordx2 a[22:23], v96, s[88:91], s81 offen offset:512 nt\n"
      "buffer_load_dwordx2 a[30:31], v96, s[88:91], s81 offen offset:640 nt\n"
      "v_smfmac_f32_16x16x64_bf16 v[44:47], v[12:15], v[60:67], v1\n"
      "ds_read_b128 v[60:63], v2 offset:18432\n"
      "s_nop 0\n"
      "v_add_f32 v30, v30, v31\n"
      "v_smfmac_f32_16x16x64_bf16 v[44:47], v[16:19], v[184:191], v1\n"
      "ds_read_b128 v[64:67], v2 offset:19456\n"
      "s_nop 0\n"
      "v_add_f32 v28, v28, v29\n"
      "v_smfmac_f32_16x16x64_bf16 v[44:47], v[20:23], v[68:75], v1\n"
      "ds_read_b128 v[68:71], v2 offset:20480\n"
      "s_nop 0\n"
      "v_add_f32 v34, v34, v35\n"
      "v_smfmac_f32_16x16x64_bf16 v[44:47], v[24:27], v[192:199], v1\n"
      "ds_read_b128 v[72:75], v2 offset:21504\n"
      "s_nop 0\n"
      "v_add_f32_dpp v29, v30, v28 quad_perm:[1,0,3,2] row_mask:0xf bank_mask:0xf\n"
      "v_smfmac_f32_16x16x64_bf16 v[52:55], v[12:15], v[216:223], v1\n"
      "s_nop 1\n"
      "v_add_f32 v32, v32, v33\n"
      "v_smfmac_f32_16x16x64_bf16 v[52:55], v[16:19], v[76:83], v1\n"
      "ds_read_b128 v[76:79], v2 offset:22528\n"
      "s_nop 0\n"
      "v_add_f32 v36, v36, v37\n"
      "v_smfmac_f32_16x16x64_bf16 v[52:55], v[20:23], v[224:231], v1\n"
      "ds_read_b128 v[80:83], v2 offset:23552\n"
      "s_waitcnt lgkmcnt(4)\n"
      "v_add_f32_dpp v33, v34, v32 quad_perm:[1,0,3,2] row_mask:0xf bank_mask:0xf\n"
      "v_smfmac_f32_16x16x64_bf16 v[52:55], v[24:27], v[60:67], v1\n"
      "ds_read_b128 v[60:63], v2 offset:24576\n"
      "s_memrealtime s[6:7]\n"
      "s_waitcnt lgkmcnt(0)\n"
      "s_sub_u32 s6, s6, s4\n"
      "s_lshl_b32 s6, s6, 5\n"
      "s_sub_u32 s6, s5, s6\n"
      "s_cmp_lt_i32 s6, 640\n"
      "s_cbranch_scc1 .Lk_pc_53\n"
      "s_sleep 5\n"
      ".Lk_pc_53:\n"
      "s_memrealtime s[6:7]\n"
      "s_waitcnt lgkmcnt(0)\n"
      "s_sub_u32 s6, s6, s4\n"
      "s_lshl_b32 s6, s6, 5\n"
      "s_cmp_lt_i32 s6, s5\n"
      "s_cbranch_scc1 .Lk_pc_53\n"
      "s_add_u32 s5, s5, s30\n"
      "s_xor_b32 s81, s83, 0x6400\n"
      "buffer_load_dwordx2 a[38:39], v96, s[88:91], s81 offen nt\n"
      "buffer_load_dwordx2 a[46:47], v96, s[88:91], s81 offen offset:128 nt\n"
      "buffer_load_dwordx2 a[54:55], v96, s[88:91], s81 offen offset:512 nt\n"
      "buffer_load_dwordx2 a[62:63], v96, s[88:91], s81 offen offset:640 nt\n"
      "v_add_f32 v38, v38, v39\n"
      "v_smfmac_f32_16x16x64_bf16 v[48:51], v[12:15], v[200:207], v1\n"
      "ds_read_b128 v[64:67], v2 offset:25600\n"
      "s_nop 0\n"
      "v_add_f32 v40, v40, v41\n"
      "v_smfmac_f32_16x16x64_bf16 v[48:51], v[16:19], v[68:75], v1\n"
      "s_nop 1\n"
      "v_add_f32_dpp v37, v36, v38 quad_perm:[1,0,3,2] row_mask:0xf bank_mask:0xf\n"
      "v_smfmac_f32_16x16x64_bf16 v[48:51], v[20:23], v[208:215], v1\n"
      "s_nop 1\n"
      "v_add_f32 v42, v42, v43\n"
      "v_smfmac_f32_16x16x64_bf16 v[48:51], v[24:27], v[76:83], v1\n"
      "s_nop 1\n"
      "v_cndmask_b32 v84, v29, v37, s[64:65]\n"
      "v_smfmac_f32_16x16x64_bf16 v[56:59], v[12:15], v[232:239], v1\n"
      "s_waitcnt lgkmcnt(0)\n"
      "s_nop 0\n"
      "v_add_f32_dpp v41, v40, v42 quad_perm:[1,0,3,2] row_mask:0xf bank_mask:0xf\n"
      "v_smfmac_f32_16x16x64_bf16 v[56:59], v[16:19], v[60:67], v1\n"
      "s_nop 1\n"
      "v_add_f32 v46, v46, v47\n"
      "v_smfmac_f32_16x16x64_bf16 v[56:59], v[20:23], v[240:247], v1\n"
      "s_nop 1\n"
      "v_cndmask_b32 v85, v33, v41, s[64:65]\n"
      "v_smfmac_f32_16x16x64_bf16 v[56:59], v[24:27], v[248:255], v1\n"
      "s_memrealtime s[6:7]\n"
      "s_waitcnt lgkmcnt(0)\n"
      "s_sub_u32 s6, s6, s4\n"
      "s_lshl_b32 s6, s6, 5\n"
      "s_sub_u32 s6, s5, s6\n"
      "s_cmp_lt_i32 s6, 640\n"
      "s_cbranch_scc1 .Lk_pc_54\n"
      "s_sleep 5\n"
      ".Lk_pc_54:\n"
      "s_memrealtime s[6:7]\n"
      "s_waitcnt lgkmcnt(0)\n"
      "s_sub_u32 s6, s6, s4\n"
      "s_lshl_b32 s6, s6, 5\n"
      "s_cmp_lt_i32 s6, s5\n"
      "s_cbranch_scc1 .Lk_pc_54\n"
      "s_add_u32 s5, s5, s30\n"
      "s_xor_b32 s81, s83, 0x20000\n"
      "buffer_load_dwordx2 a[64:65], v96, s[88:91], s81 offen nt\n"
      "buffer_load_dwordx2 a[72:73], v96, s[88:91], s81 offen offset:128 nt\n"
      "buffer_load_dwordx2 a[80:81], v96, s[88:91], s81 offen offset:512 nt\n"
      "buffer_load_dwordx2 a[88:89], v96, s[88:91], s81 offen offset:640 nt\n"
      "v_add_f32 v44, v44, v45\n"
      "v_add_f32 v52, v52, v53\n"
      "v_add_f32 v54, v54, v55\n"
      "v_add_f32_dpp v45, v46, v44 quad_perm:[1,0,3,2] row_mask:0xf bank_mask:0xf\n"
      "s_nop 0\n"
      "v_add_f32_dpp v53, v52, v54 quad_perm:[1,0,3,2] row_mask:0xf bank_mask:0xf\n"
      "v_add_f32 v50, v50, v51\n"
      "v_add_f32 v48, v48, v49\n"
      "v_cndmask_b32 v86, v45, v53, s[64:65]\n"
      "s_nop 0\n"
      "v_add_f32_dpp v49, v50, v48 quad_perm:[1,0,3,2] row_mask:0xf bank_mask:0xf\n"
      "v_add_f32 v56, v56, v57\n"
      "v_add_f32 v58, v58, v59\n"
      "v_max3_f32 v88, v84, v85, v86\n"
      "s_nop 0\n"
      "v_add_f32_dpp v57, v56, v58 quad_perm:[1,0,3,2] row_mask:0xf bank_mask:0xf\n"
      "v_cndmask_b32 v87, v49, v57, s[64:65]\n"
      "v_max_f32 v88, v88, v87\n"
      "v_mul_f32 v88, v10, v88\n"
      "v_mov_b64 v[20:21], 0\n"
      "v_mov_b64 v[22:23], 0\n"
      "v_max_f32_dpp v88, v88, v88 row_ror:8 row_mask:0xf bank_mask:0xf\n"
      "v_mov_b64 v[24:25], 0\n"
      "v_mov_b64 v[26:27], 0\n"
      "v_max_f32_dpp v88, v88, v88 row_ror:4 row_mask:0xf bank_mask:0xf\n"
      "v_mul_f32 v84, v10, v84\n"
      "v_mul_f32 v85, v10, v85\n"
      "v_max_f32_dpp v88, v88, v88 row_ror:2 row_mask:0xf bank_mask:0xf\n"
      "v_mul_f32 v86, v10, v86\n"
      "v_mul_f32 v87, v10, v87\n"
      "v_max_f32_dpp v88, v88, v88 row_ror:1 row_mask:0xf bank_mask:0xf\n"
      "v_lshrrev_b32 v28, 4, v0\n"
      "v_mov_b32 v29, 162912\n"
      "v_max_f32_dpp v88, v88, v88 row_bcast:15 row_mask:0xa bank_mask:0xf\n"
      "v_lshl_add_u32 v28, v28, 5, s59\n"
      "s_nop 0\n"
      "v_max_f32_dpp v88, v88, v88 row_bcast:31 row_mask:0xc bank_mask:0xf\n"
      "s_nop 0\n"
      "v_readlane_b32 s10, v88, 63\n"
      "s_nop 1\n"
      "v_subrev_f32 v88, s10, v84\n"
      "v_subrev_f32 v89, s10, v85\n"
      "v_subrev_f32 v90, s10, v86\n"
      "v_subrev_f32 v91, s10, v87\n"
      "v_exp_f32 v88, v88\n"
      "v_exp_f32 v89, v89\n"
      "v_cvt_pk_bf16_f32 v84, v88, 0\n"
      "v_cvt_pk_bf16_f32 v85, v89, 0\n"
      "v_exp_f32 v90, v90\n"
      "v_exp_f32 v91, v91\n"
      "v_mov_b64_dpp v[12:13], v[84:85] row_newbcast:0 row_mask:0xf bank_mask:0xf\n"
      "v_mov_b64_dpp v[14:15], v[84:85] row_newbcast:2 row_mask:0xf bank_mask:0xf\n"
      "v_mov_b64_dpp v[16:17], v[84:85] row_newbcast:4 row_mask:0xf bank_mask:0xf\n"
      "s_memrealtime s[6:7]\n"
      "s_waitcnt lgkmcnt(0)\n"
      "s_sub_u32 s6, s6, s4\n"
      "s_lshl_b32 s6, s6, 5\n"
      "s_sub_u32 s6, s5, s6\n"
      "s_cmp_lt_i32 s6, 640\n"
      "s_cbranch_scc1 .Lk_pc_55\n"
      "s_sleep 5\n"
      ".Lk_pc_55:\n"
      "s_memrealtime s[6:7]\n"
      "s_waitcnt lgkmcnt(0)\n"
      "s_sub_u32 s6, s6, s4\n"
      "s_lshl_b32 s6, s6, 5\n"
      "s_cmp_lt_i32 s6, s5\n"
      "s_cbranch_scc1 .Lk_pc_55\n"
      "s_add_u32 s5, s5, s30\n"
      "s_xor_b32 s81, s83, 0x20400\n"
      "buffer_load_dwordx2 a[96:97], v96, s[88:91], s81 offen nt\n"
      "buffer_load_dwordx2 a[104:105], v96, s[88:91], s81 offen offset:128 nt\n"
      "buffer_load_dwordx2 a[112:113], v96, s[88:91], s81 offen offset:512 nt\n"
      "buffer_load_dwordx2 a[120:121], v96, s[88:91], s81 offen offset:640 nt\n"
      "s_memrealtime s[6:7]\n"
      "s_waitcnt lgkmcnt(0)\n"
      "s_sub_u32 s6, s6, s4\n"
      "s_lshl_b32 s6, s6, 5\n"
      "s_sub_u32 s6, s5, s6\n"
      "s_cmp_lt_i32 s6, 640\n"
      "s_cbranch_scc1 .Lk_pc_56\n"
      "s_sleep 5\n"
      ".Lk_pc_56:\n"
      "s_memrealtime s[6:7]\n"
      "s_waitcnt lgkmcnt(0)\n"
      "s_sub_u32 s6, s6, s4\n"
      "s_lshl_b32 s6, s6, 5\n"
      "s_cmp_lt_i32 s6, s5\n"
      "s_cbranch_scc1 .Lk_pc_56\n"
      "s_add_u32 s5, s5, s30\n"
      "s_xor_b32 s81, s83, 0x22000\n"
      "buffer_load_dwordx2 a[68:69], v96, s[88:91], s81 offen nt\n"
      "s_waitcnt vmcnt(40)\n"
      "buffer_load_dwordx2 a[76:77], v96, s[88:91], s81 offen offset:128 nt\n"
      "s_waitcnt vmcnt(40)\n"
      "buffer_load_dwordx2 a[84:85], v96, s[88:91], s81 offen offset:512 nt\n"
      "s_waitcnt vmcnt(40)\n"
      "buffer_load_dwordx2 a[92:93], v96, s[88:91], s81 offen offset:640 nt\n"
      "s_waitcnt vmcnt(40)\n"
      "s_memrealtime s[6:7]\n"
      "s_waitcnt lgkmcnt(0)\n"
      "s_sub_u32 s6, s6, s4\n"
      "s_lshl_b32 s6, s6, 5\n"
      "s_sub_u32 s6, s5, s6\n"
      "s_cmp_lt_i32 s6, 640\n"
      "s_cbranch_scc1 .Lk_pc_57\n"
      "s_sleep 5\n"
      ".Lk_pc_57:\n"
      "s_memrealtime s[6:7]\n"
      "s_waitcnt lgkmcnt(0)\n"
      "s_sub_u32 s6, s6, s4\n"
      "s_lshl_b32 s6, s6, 5\n"
      "s_cmp_lt_i32 s6, s5\n"
      "s_cbranch_scc1 .Lk_pc_57\n"
      "s_add_u32 s5, s5, s30\n"
      "s_xor_b32 s81, s83, 0x22400\n"
      "buffer_load_dwordx2 a[100:101], v96, s[88:91], s81 offen nt\n"
      "s_waitcnt vmcnt(40)\n"
      "buffer_load_dwordx2 a[108:109], v96, s[88:91], s81 offen offset:128 nt\n"
      "s_waitcnt vmcnt(40)\n"
      "buffer_load_dwordx2 a[116:117], v96, s[88:91], s81 offen offset:512 nt\n"
      "s_waitcnt vmcnt(40)\n"
      "buffer_load_dwordx2 a[124:125], v96, s[88:91], s81 offen offset:640 nt\n"
      "s_waitcnt vmcnt(40)\n"
      "s_memrealtime s[6:7]\n"
      "s_waitcnt lgkmcnt(0)\n"
      "s_sub_u32 s6, s6, s4\n"
      "s_lshl_b32 s6, s6, 5\n"
      "s_sub_u32 s6, s5, s6\n"
      "s_cmp_lt_i32 s6, 640\n"
      "s_cbranch_scc1 .Lk_pc_58\n"
      "s_sleep 5\n"
      ".Lk_pc_58:\n"
      "s_memrealtime s[6:7]\n"
      "s_waitcnt lgkmcnt(0)\n"
      "s_sub_u32 s6, s6, s4\n"
      "s_lshl_b32 s6, s6, 5\n"
      "s_cmp_lt_i32 s6, s5\n"
      "s_cbranch_scc1 .Lk_pc_58\n"
      "s_add_u32 s5, s5, s30\n"
      "s_xor_b32 s81, s83, 0x24000\n"
      "buffer_load_dwordx2 a[66:67], v96, s[88:91], s81 offen nt\n"
      "s_waitcnt vmcnt(40)\n"
      "buffer_load_dwordx2 a[74:75], v96, s[88:91], s81 offen offset:128 nt\n"
      "s_waitcnt vmcnt(40)\n"
      "buffer_load_dwordx2 a[82:83], v96, s[88:91], s81 offen offset:512 nt\n"
      "s_waitcnt vmcnt(40)\n"
      "buffer_load_dwordx2 a[90:91], v96, s[88:91], s81 offen offset:640 nt\n"
      "s_waitcnt vmcnt(40)\n"
      "s_waitcnt vmcnt(26)\n"
      "v_smfmac_f32_16x16x64_bf16 v[20:23], v[12:15], a[0:7], v1 abid:1\n"
      "v_cvt_pk_bf16_f32 v86, v90, 0\n"
      "v_mov_b64_dpp v[18:19], v[84:85] row_newbcast:6 row_mask:0xf bank_mask:0xf\n"
      "v_smfmac_f32_16x16x64_bf16 v[24:27], v[12:15], a[8:15], v1 abid:1\n"
      "v_cvt_pk_bf16_f32 v87, v91, 0\n"
      "v_mov_b64_dpp v[12:13], v[84:85] row_newbcast:8 row_mask:0xf bank_mask:0xf\n"
      "v_mov_b32 v62, 0x40000000\n"
      "s_waitcnt vmcnt(24)\n"
      "v_smfmac_f32_16x16x64_bf16 v[20:23], v[16:19], a[16:23], v1 abid:1\n"
      "v_mov_b32 v92, s10\n"
      "v_mov_b64_dpp v[14:15], v[84:85] row_newbcast:10 row_mask:0xf bank_mask:0xf\n"
      "v_smfmac_f32_16x16x64_bf16 v[24:27], v[16:19], a[24:31], v1 abid:1\n"
      "v_add_f32 v88, v88, v89\n"
      "v_mov_b64_dpp v[16:17], v[84:85] row_newbcast:12 row_mask:0xf bank_mask:0xf\n"
      "s_waitcnt vmcnt(22)\n"
      "v_smfmac_f32_16x16x64_bf16 v[20:23], v[12:15], a[32:39], v1 abid:1\n"
      "v_add_f32 v90, v90, v91\n"
      "v_mov_b64_dpp v[18:19], v[84:85] row_newbcast:14 row_mask:0xf bank_mask:0xf\n"
      "v_smfmac_f32_16x16x64_bf16 v[24:27], v[12:15], a[40:47], v1 abid:1\n"
      "v_add_f32 v93, v88, v90\n"
      "v_mov_b64_dpp v[12:13], v[84:85] row_newbcast:1 row_mask:0xf bank_mask:0xf\n"
      "s_waitcnt vmcnt(20)\n"
      "v_smfmac_f32_16x16x64_bf16 v[20:23], v[16:19], a[48:55], v1 abid:1\n"
      "v_add_f32_dpp v93, v93, v93 row_ror:8 row_mask:0xf bank_mask:0xf\n"
      "v_mov_b64_dpp v[14:15], v[84:85] row_newbcast:3 row_mask:0xf bank_mask:0xf\n"
      "v_smfmac_f32_16x16x64_bf16 v[24:27], v[16:19], a[56:63], v1 abid:1\n"
      "v_add_f32_dpp v93, v93, v93 row_ror:4 row_mask:0xf bank_mask:0xf\n"
      "v_mov_b64_dpp v[16:17], v[84:85] row_newbcast:5 row_mask:0xf bank_mask:0xf\n"
      "s_memrealtime s[6:7]\n"
      "s_waitcnt lgkmcnt(0)\n"
      "s_sub_u32 s6, s6, s4\n"
      "s_lshl_b32 s6, s6, 5\n"
      "s_sub_u32 s6, s5, s6\n"
      "s_cmp_lt_i32 s6, 640\n"
      "s_cbranch_scc1 .Lk_pc_59\n"
      "s_sleep 5\n"
      ".Lk_pc_59:\n"
      "s_memrealtime s[6:7]\n"
      "s_waitcnt lgkmcnt(0)\n"
      "s_sub_u32 s6, s6, s4\n"
      "s_lshl_b32 s6, s6, 5\n"
      "s_cmp_lt_i32 s6, s5\n"
      "s_cbranch_scc1 .Lk_pc_59\n"
      "s_add_u32 s5, s5, s30\n"
      "s_xor_b32 s81, s83, 0x24400\n"
      "buffer_load_dwordx2 a[98:99], v96, s[88:91], s81 offen nt\n"
      "buffer_load_dwordx2 a[106:107], v96, s[88:91], s81 offen offset:128 nt\n"
      "buffer_load_dwordx2 a[114:115], v96, s[88:91], s81 offen offset:512 nt\n"
      "buffer_load_dwordx2 a[122:123], v96, s[88:91], s81 offen offset:640 nt\n"
      "s_memrealtime s[6:7]\n"
      "s_waitcnt lgkmcnt(0)\n"
      "s_sub_u32 s6, s6, s4\n"
      "s_lshl_b32 s6, s6, 5\n"
      "s_sub_u32 s6, s5, s6\n"
      "s_cmp_lt_i32 s6, 640\n"
      "s_cbranch_scc1 .Lk_pc_60\n"
      "s_sleep 5\n"
      ".Lk_pc_60:\n"
      "s_memrealtime s[6:7]\n"
      "s_waitcnt lgkmcnt(0)\n"
      "s_sub_u32 s6, s6, s4\n"
      "s_lshl_b32 s6, s6, 5\n"
      "s_cmp_lt_i32 s6, s5\n"
      "s_cbranch_scc1 .Lk_pc_60\n"
      "s_add_u32 s5, s5, s30\n"
      "s_xor_b32 s81, s83, 0x26000\n"
      "buffer_load_dwordx2 a[70:71], v96, s[88:91], s81 offen nt\n"
      "buffer_load_dwordx2 a[78:79], v96, s[88:91], s81 offen offset:128 nt\n"
      "buffer_load_dwordx2 a[86:87], v96, s[88:91], s81 offen offset:512 nt\n"
      "buffer_load_dwordx2 a[94:95], v96, s[88:91], s81 offen offset:640 nt\n"
      "s_memrealtime s[6:7]\n"
      "s_waitcnt lgkmcnt(0)\n"
      "s_sub_u32 s6, s6, s4\n"
      "s_lshl_b32 s6, s6, 5\n"
      "s_sub_u32 s6, s5, s6\n"
      "s_cmp_lt_i32 s6, 640\n"
      "s_cbranch_scc1 .Lk_pc_61\n"
      "s_sleep 5\n"
      ".Lk_pc_61:\n"
      "s_memrealtime s[6:7]\n"
      "s_waitcnt lgkmcnt(0)\n"
      "s_sub_u32 s6, s6, s4\n"
      "s_lshl_b32 s6, s6, 5\n"
      "s_cmp_lt_i32 s6, s5\n"
      "s_cbranch_scc1 .Lk_pc_61\n"
      "s_add_u32 s5, s5, s30\n"
      "s_xor_b32 s81, s83, 0x26400\n"
      "buffer_load_dwordx2 a[102:103], v96, s[88:91], s81 offen nt\n"
      "buffer_load_dwordx2 a[110:111], v96, s[88:91], s81 offen offset:128 nt\n"
      "buffer_load_dwordx2 a[118:119], v96, s[88:91], s81 offen offset:512 nt\n"
      "buffer_load_dwordx2 a[126:127], v96, s[88:91], s81 offen offset:640 nt\n"
      "s_memrealtime s[6:7]\n"
      "s_waitcnt lgkmcnt(0)\n"
      "s_sub_u32 s6, s6, s4\n"
      "s_lshl_b32 s6, s6, 5\n"
      "s_sub_u32 s6, s5, s6\n"
      "s_cmp_lt_i32 s6, 640\n"
      "s_cbranch_scc1 .Lk_pc_62\n"
      "s_sleep 5\n"
      ".Lk_pc_62:\n"
      "s_memrealtime s[6:7]\n"
      "s_waitcnt lgkmcnt(0)\n"
      "s_sub_u32 s6, s6, s4\n"
      "s_lshl_b32 s6, s6, 5\n"
      "s_cmp_lt_i32 s6, s5\n"
      "s_cbranch_scc1 .Lk_pc_62\n"
      "s_add_u32 s5, s5, s30\n"
      "s_xor_b32 s81, s83, 0x1000\n"
      "buffer_load_dwordx2 a[128:129], v96, s[88:91], s81 offen nt\n"
      "buffer_load_dwordx2 a[136:137], v96, s[88:91], s81 offen offset:128 nt\n"
      "buffer_load_dwordx2 a[144:145], v96, s[88:91], s81 offen offset:512 nt\n"
      "buffer_load_dwordx2 a[152:153], v96, s[88:91], s81 offen offset:640 nt\n"
      "s_memrealtime s[6:7]\n"
      "s_waitcnt lgkmcnt(0)\n"
      "s_sub_u32 s6, s6, s4\n"
      "s_lshl_b32 s6, s6, 5\n"
      "s_sub_u32 s6, s5, s6\n"
      "s_cmp_lt_i32 s6, 640\n"
      "s_cbranch_scc1 .Lk_pc_63\n"
      "s_sleep 5\n"
      ".Lk_pc_63:\n"
      "s_memrealtime s[6:7]\n"
      "s_waitcnt lgkmcnt(0)\n"
      "s_sub_u32 s6, s6, s4\n"
      "s_lshl_b32 s6, s6, 5\n"
      "s_cmp_lt_i32 s6, s5\n"
      "s_cbranch_scc1 .Lk_pc_63\n"
      "s_add_u32 s5, s5, s30\n"
      "s_xor_b32 s81, s83, 0x1400\n"
      "buffer_load_dwordx2 a[160:161], v96, s[88:91], s81 offen nt\n"
      "buffer_load_dwordx2 a[168:169], v96, s[88:91], s81 offen offset:128 nt\n"
      "buffer_load_dwordx2 a[176:177], v96, s[88:91], s81 offen offset:512 nt\n"
      "buffer_load_dwordx2 a[184:185], v96, s[88:91], s81 offen offset:640 nt\n"
      "s_memrealtime s[6:7]\n"
      "s_waitcnt lgkmcnt(0)\n"
      "s_sub_u32 s6, s6, s4\n"
      "s_lshl_b32 s6, s6, 5\n"
      "s_sub_u32 s6, s5, s6\n"
      "s_cmp_lt_i32 s6, 640\n"
      "s_cbranch_scc1 .Lk_pc_64\n"
      "s_sleep 5\n"
      ".Lk_pc_64:\n"
      "s_memrealtime s[6:7]\n"
      "s_waitcnt lgkmcnt(0)\n"
      "s_sub_u32 s6, s6, s4\n"
      "s_lshl_b32 s6, s6, 5\n"
      "s_cmp_lt_i32 s6, s5\n"
      "s_cbranch_scc1 .Lk_pc_64\n"
      "s_add_u32 s5, s5, s30\n"
      "s_xor_b32 s81, s83, 0x3000\n"
      "buffer_load_dwordx2 a[132:133], v96, s[88:91], s81 offen nt\n"
      "s_waitcnt vmcnt(40)\n"
      "buffer_load_dwordx2 a[140:141], v96, s[88:91], s81 offen offset:128 nt\n"
      "s_waitcnt vmcnt(40)\n"
      "buffer_load_dwordx2 a[148:149], v96, s[88:91], s81 offen offset:512 nt\n"
      "s_waitcnt vmcnt(40)\n"
      "buffer_load_dwordx2 a[156:157], v96, s[88:91], s81 offen offset:640 nt\n"
      "s_waitcnt vmcnt(40)\n"
      "s_memrealtime s[6:7]\n"
      "s_waitcnt lgkmcnt(0)\n"
      "s_sub_u32 s6, s6, s4\n"
      "s_lshl_b32 s6, s6, 5\n"
      "s_sub_u32 s6, s5, s6\n"
      "s_cmp_lt_i32 s6, 640\n"
      "s_cbranch_scc1 .Lk_pc_65\n"
      "s_sleep 5\n"
      ".Lk_pc_65:\n"
      "s_memrealtime s[6:7]\n"
      "s_waitcnt lgkmcnt(0)\n"
      "s_sub_u32 s6, s6, s4\n"
      "s_lshl_b32 s6, s6, 5\n"
      "s_cmp_lt_i32 s6, s5\n"
      "s_cbranch_scc1 .Lk_pc_65\n"
      "s_add_u32 s5, s5, s30\n"
      "s_xor_b32 s81, s83, 0x3400\n"
      "buffer_load_dwordx2 a[164:165], v96, s[88:91], s81 offen nt\n"
      "s_waitcnt vmcnt(40)\n"
      "buffer_load_dwordx2 a[172:173], v96, s[88:91], s81 offen offset:128 nt\n"
      "s_waitcnt vmcnt(40)\n"
      "buffer_load_dwordx2 a[180:181], v96, s[88:91], s81 offen offset:512 nt\n"
      "s_waitcnt vmcnt(40)\n"
      "buffer_load_dwordx2 a[188:189], v96, s[88:91], s81 offen offset:640 nt\n"
      "s_waitcnt vmcnt(40)\n"
      "s_memrealtime s[6:7]\n"
      "s_waitcnt lgkmcnt(0)\n"
      "s_sub_u32 s6, s6, s4\n"
      "s_lshl_b32 s6, s6, 5\n"
      "s_sub_u32 s6, s5, s6\n"
      "s_cmp_lt_i32 s6, 640\n"
      "s_cbranch_scc1 .Lk_pc_66\n"
      "s_sleep 5\n"
      ".Lk_pc_66:\n"
      "s_memrealtime s[6:7]\n"
      "s_waitcnt lgkmcnt(0)\n"
      "s_sub_u32 s6, s6, s4\n"
      "s_lshl_b32 s6, s6, 5\n"
      "s_cmp_lt_i32 s6, s5\n"
      "s_cbranch_scc1 .Lk_pc_66\n"
      "s_add_u32 s5, s5, s30\n"
      "s_xor_b32 s81, s83, 0x5000\n"
      "buffer_load_dwordx2 a[130:131], v96, s[88:91], s81 offen nt\n"
      "s_waitcnt vmcnt(40)\n"
      "buffer_load_dwordx2 a[138:139], v96, s[88:91], s81 offen offset:128 nt\n"
      "s_waitcnt vmcnt(40)\n"
      "buffer_load_dwordx2 a[146:147], v96, s[88:91], s81 offen offset:512 nt\n"
      "s_waitcnt vmcnt(40)\n"
      "buffer_load_dwordx2 a[154:155], v96, s[88:91], s81 offen offset:640 nt\n"
      "s_waitcnt vmcnt(40)\n"
      "s_waitcnt vmcnt(26)\n"
      "v_smfmac_f32_16x16x64_bf16 v[20:23], v[12:15], a[64:71], v1 abid:1\n"
      "v_add_f32_dpp v93, v93, v93 row_ror:2 row_mask:0xf bank_mask:0xf\n"
      "v_mov_b64_dpp v[18:19], v[84:85] row_newbcast:7 row_mask:0xf bank_mask:0xf\n"
      "v_smfmac_f32_16x16x64_bf16 v[24:27], v[12:15], a[72:79], v1 abid:1\n"
      "v_add_f32_dpp v93, v93, v93 row_ror:1 row_mask:0xf bank_mask:0xf\n"
      "v_mov_b64_dpp v[12:13], v[84:85] row_newbcast:9 row_mask:0xf bank_mask:0xf\n"
      "s_cmp_lg_u32 s61, 2\n"
      "s_cbranch_scc1 .Lk_cs_67\n"
      "s_and_b32 s68, s75, 0x1\n"
      "s_lshl_b32 s68, s68, 6\n"
      "s_add_u32 s68, s68, 0x10000\n"
      "v_bfe_u32 v11, v0, 4, 2\n"
      "v_lshl_add_u32 v66, v11, 14, s68\n"
      "v_bfe_u32 v11, v0, 2, 2\n"
      "v_lshl_add_u32 v66, v11, 11, v66\n"
      "v_bfe_u32 v11, v0, 0, 2\n"
      "v_lshl_add_u32 v66, v11, 4, v66\n"
      "s_and_b32 s68, s75, 0x1e\n"
      "s_lshr_b32 s68, s68, 1\n"
      "v_bfe_u32 v11, v0, 2, 4\n"
      "v_lshl_add_u32 v67, v11, 0, s68\n"
      "v_and_b32 v67, 15, v67\n"
      "v_lshl_add_u32 v66, v67, 7, v66\n"
      "s_mov_b32 s78, 0xffffff\n"
      "s_mov_b32 s79, 0x0\n"
      "v_mov_b32 v11, 0x40000000\n"
      "v_cndmask_b32 v67, v11, v66, s[78:79]\n"
      "s_and_b32 s68, s57, 0x1c\n"
      "s_lshl_b32 s68, s68, 13\n"
      "s_and_b32 s79, s57, 0x20\n"
      "s_lshl_b32 s79, s79, 9\n"
      "s_add_u32 s68, s68, s79\n"
      "s_add_u32 s68, s68, 0x130000\n"
      "s_and_b32 s76, s57, 0x1c\n"
      "s_lshl_b32 s76, s76, 12\n"
      "s_add_u32 s76, s76, 0x100000\n"
      "s_bitcmp1_b32 s57, 6\n"
      "s_cselect_b32 s68, s76, s68\n"
      "s_add_u32 s68, s68, s63\n"
      "s_and_b32 s76, s57, 0x1c\n"
      "s_lshl_b32 s76, s76, 11\n"
      "s_add_u32 s76, s76, 0x120000\n"
      "s_and_b32 s77, s57, 0x60\n"
      "s_lshr_b32 s77, s77, 5\n"
      "s_cmp_eq_u32 s77, 3\n"
      "s_cselect_b32 s68, s76, s68\n"
      "s_and_b32 s76, s57, 0x3\n"
      "s_lshl_b32 s76, s76, 11\n"
      "s_add_u32 s68, s68, s76\n"
      "v_lshl_add_u32 v68, v0, 4, s68\n"
      "v_mov_b32 v70, -1\n"
      "v_mov_b32 v71, -1\n"
      "v_mov_b32 v72, -1\n"
      "v_mov_b32 v73, -1\n"
      "s_bitcmp1_b32 s56, 0\n"
      "s_cselect_b64 s[78:79], -1, 0\n"
      "s_cselect_b32 s76, 0x30000, 0\n"
      "v_cndmask_b32 v67, v66, v67, s[78:79]\n"
      "s_bitcmp1_b32 s56, 1\n"
      "s_cselect_b32 s77, 622592, 0\n"
      "s_add_u32 s76, s76, s77\n"
      "s_add_u32 s82, s76, s63\n"
      "s_nop 0\n"
      ".Lk_cs_67:\n"
      "s_waitcnt vmcnt(24)\n"
      "v_smfmac_f32_16x16x64_bf16 v[20:23], v[16:19], a[80:87], v1 abid:1\n"
      "v_add_f32_dpp v93, v93, v93 row_bcast:15 row_mask:0xa bank_mask:0xf\n"
      "v_mov_b64_dpp v[14:15], v[84:85] row_newbcast:11 row_mask:0xf bank_mask:0xf\n"
      "v_smfmac_f32_16x16x64_bf16 v[24:27], v[16:19], a[88:95], v1 abid:1\n"
      "v_add_f32_dpp v93, v93, v93 row_bcast:31 row_mask:0xc bank_mask:0xf\n"
      "v_mov_b64_dpp v[16:17], v[84:85] row_newbcast:13 row_mask:0xf bank_mask:0xf\n"
      "s_waitcnt vmcnt(22)\n"
      "v_smfmac_f32_16x16x64_bf16 v[20:23], v[12:15], a[96:103], v1 abid:1\n"
      "ds_write2_b32 v28, v92, v93 offset0:0 offset1:4\n"
      "v_mov_b64_dpp v[18:19], v[84:85] row_newbcast:15 row_mask:0xf bank_mask:0xf\n"
      "v_smfmac_f32_16x16x64_bf16 v[24:27], v[12:15], a[104:111], v1 abid:1\n"
      "v_mov_b64_dpp v[12:13], v[86:87] row_newbcast:0 row_mask:0xf bank_mask:0xf\n"
      "s_waitcnt vmcnt(20)\n"
      "v_smfmac_f32_16x16x64_bf16 v[20:23], v[16:19], a[112:119], v1 abid:1\n"
      "v_mov_b64_dpp v[14:15], v[86:87] row_newbcast:2 row_mask:0xf bank_mask:0xf\n"
      "v_smfmac_f32_16x16x64_bf16 v[24:27], v[16:19], a[120:127], v1 abid:1\n"
      "v_mov_b64_dpp v[16:17], v[86:87] row_newbcast:4 row_mask:0xf bank_mask:0xf\n"
      "s_memrealtime s[6:7]\n"
      "s_waitcnt lgkmcnt(0)\n"
      "s_sub_u32 s6, s6, s4\n"
      "s_lshl_b32 s6, s6, 5\n"
      "s_sub_u32 s6, s5, s6\n"
      "s_cmp_lt_i32 s6, 640\n"
      "s_cbranch_scc1 .Lk_pc_68\n"
      "s_sleep 5\n"
      ".Lk_pc_68:\n"
      "s_memrealtime s[6:7]\n"
      "s_waitcnt lgkmcnt(0)\n"
      "s_sub_u32 s6, s6, s4\n"
      "s_lshl_b32 s6, s6, 5\n"
      "s_cmp_lt_i32 s6, s5\n"
      "s_cbranch_scc1 .Lk_pc_68\n"
      "s_add_u32 s5, s5, s30\n"
      "s_xor_b32 s81, s83, 0x5400\n"
      "buffer_load_dwordx2 a[162:163], v96, s[88:91], s81 offen nt\n"
      "buffer_load_dwordx2 a[170:171], v96, s[88:91], s81 offen offset:128 nt\n"
      "buffer_load_dwordx2 a[178:179], v96, s[88:91], s81 offen offset:512 nt\n"
      "buffer_load_dwordx2 a[186:187], v96, s[88:91], s81 offen offset:640 nt\n"
      "s_memrealtime s[6:7]\n"
      "s_waitcnt lgkmcnt(0)\n"
      "s_sub_u32 s6, s6, s4\n"
      "s_lshl_b32 s6, s6, 5\n"
      "s_sub_u32 s6, s5, s6\n"
      "s_cmp_lt_i32 s6, 640\n"
      "s_cbranch_scc1 .Lk_pc_69\n"
      "s_sleep 5\n"
      ".Lk_pc_69:\n"
      "s_memrealtime s[6:7]\n"
      "s_waitcnt lgkmcnt(0)\n"
      "s_sub_u32 s6, s6, s4\n"
      "s_lshl_b32 s6, s6, 5\n"
      "s_cmp_lt_i32 s6, s5\n"
      "s_cbranch_scc1 .Lk_pc_69\n"
      "s_add_u32 s5, s5, s30\n"
      "s_xor_b32 s81, s83, 0x7000\n"
      "buffer_load_dwordx2 a[134:135], v96, s[88:91], s81 offen nt\n"
      "buffer_load_dwordx2 a[142:143], v96, s[88:91], s81 offen offset:128 nt\n"
      "buffer_load_dwordx2 a[150:151], v96, s[88:91], s81 offen offset:512 nt\n"
      "buffer_load_dwordx2 a[158:159], v96, s[88:91], s81 offen offset:640 nt\n"
      "s_memrealtime s[6:7]\n"
      "s_waitcnt lgkmcnt(0)\n"
      "s_sub_u32 s6, s6, s4\n"
      "s_lshl_b32 s6, s6, 5\n"
      "s_sub_u32 s6, s5, s6\n"
      "s_cmp_lt_i32 s6, 640\n"
      "s_cbranch_scc1 .Lk_pc_70\n"
      "s_sleep 5\n"
      ".Lk_pc_70:\n"
      "s_memrealtime s[6:7]\n"
      "s_waitcnt lgkmcnt(0)\n"
      "s_sub_u32 s6, s6, s4\n"
      "s_lshl_b32 s6, s6, 5\n"
      "s_cmp_lt_i32 s6, s5\n"
      "s_cbranch_scc1 .Lk_pc_70\n"
      "s_add_u32 s5, s5, s30\n"
      "s_xor_b32 s81, s83, 0x7400\n"
      "buffer_load_dwordx2 a[166:167], v96, s[88:91], s81 offen nt\n"
      "buffer_load_dwordx2 a[174:175], v96, s[88:91], s81 offen offset:128 nt\n"
      "buffer_load_dwordx2 a[182:183], v96, s[88:91], s81 offen offset:512 nt\n"
      "buffer_load_dwordx2 a[190:191], v96, s[88:91], s81 offen offset:640 nt\n"
      "s_memrealtime s[6:7]\n"
      "s_waitcnt lgkmcnt(0)\n"
      "s_sub_u32 s6, s6, s4\n"
      "s_lshl_b32 s6, s6, 5\n"
      "s_sub_u32 s6, s5, s6\n"
      "s_cmp_lt_i32 s6, 640\n"
      "s_cbranch_scc1 .Lk_pc_71\n"
      "s_sleep 5\n"
      ".Lk_pc_71:\n"
      "s_memrealtime s[6:7]\n"
      "s_waitcnt lgkmcnt(0)\n"
      "s_sub_u32 s6, s6, s4\n"
      "s_lshl_b32 s6, s6, 5\n"
      "s_cmp_lt_i32 s6, s5\n"
      "s_cbranch_scc1 .Lk_pc_71\n"
      "s_add_u32 s5, s5, s30\n"
      "s_xor_b32 s81, s83, 0x21000\n"
      "buffer_load_dwordx2 a[192:193], v96, s[88:91], s81 offen nt\n"
      "buffer_load_dwordx2 a[200:201], v96, s[88:91], s81 offen offset:128 nt\n"
      "buffer_load_dwordx2 a[208:209], v96, s[88:91], s81 offen offset:512 nt\n"
      "buffer_load_dwordx2 a[216:217], v96, s[88:91], s81 offen offset:640 nt\n"
      "s_memrealtime s[6:7]\n"
      "s_waitcnt lgkmcnt(0)\n"
      "s_sub_u32 s6, s6, s4\n"
      "s_lshl_b32 s6, s6, 5\n"
      "s_sub_u32 s6, s5, s6\n"
      "s_cmp_lt_i32 s6, 640\n"
      "s_cbranch_scc1 .Lk_pc_72\n"
      "s_sleep 5\n"
      ".Lk_pc_72:\n"
      "s_memrealtime s[6:7]\n"
      "s_waitcnt lgkmcnt(0)\n"
      "s_sub_u32 s6, s6, s4\n"
      "s_lshl_b32 s6, s6, 5\n"
      "s_cmp_lt_i32 s6, s5\n"
      "s_cbranch_scc1 .Lk_pc_72\n"
      "s_add_u32 s5, s5, s30\n"
      "s_xor_b32 s81, s83, 0x21400\n"
      "buffer_load_dwordx2 a[224:225], v96, s[88:91], s81 offen nt\n"
      "buffer_load_dwordx2 a[232:233], v96, s[88:91], s81 offen offset:128 nt\n"
      "buffer_load_dwordx2 a[240:241], v96, s[88:91], s81 offen offset:512 nt\n"
      "buffer_load_dwordx2 a[248:249], v96, s[88:91], s81 offen offset:640 nt\n"
      "s_memrealtime s[6:7]\n"
      "s_waitcnt lgkmcnt(0)\n"
      "s_sub_u32 s6, s6, s4\n"
      "s_lshl_b32 s6, s6, 5\n"
      "s_sub_u32 s6, s5, s6\n"
      "s_cmp_lt_i32 s6, 640\n"
      "s_cbranch_scc1 .Lk_pc_73\n"
      "s_sleep 5\n"
      ".Lk_pc_73:\n"
      "s_memrealtime s[6:7]\n"
      "s_waitcnt lgkmcnt(0)\n"
      "s_sub_u32 s6, s6, s4\n"
      "s_lshl_b32 s6, s6, 5\n"
      "s_cmp_lt_i32 s6, s5\n"
      "s_cbranch_scc1 .Lk_pc_73\n"
      "s_add_u32 s5, s5, s30\n"
      "s_xor_b32 s81, s83, 0x23000\n"
      "buffer_load_dwordx2 a[196:197], v96, s[88:91], s81 offen nt\n"
      "s_waitcnt vmcnt(40)\n"
      "buffer_load_dwordx2 a[204:205], v96, s[88:91], s81 offen offset:128 nt\n"
      "s_waitcnt vmcnt(40)\n"
      "buffer_load_dwordx2 a[212:213], v96, s[88:91], s81 offen offset:512 nt\n"
      "s_waitcnt vmcnt(40)\n"
      "buffer_load_dwordx2 a[220:221], v96, s[88:91], s81 offen offset:640 nt\n"
      "s_waitcnt vmcnt(40)\n"
      "s_memrealtime s[6:7]\n"
      "s_waitcnt lgkmcnt(0)\n"
      "s_sub_u32 s6, s6, s4\n"
      "s_lshl_b32 s6, s6, 5\n"
      "s_sub_u32 s6, s5, s6\n"
      "s_cmp_lt_i32 s6, 640\n"
      "s_cbranch_scc1 .Lk_pc_74\n"
      "s_sleep 5\n"
      ".Lk_pc_74:\n"
      "s_memrealtime s[6:7]\n"
      "s_waitcnt lgkmcnt(0)\n"
      "s_sub_u32 s6, s6, s4\n"
      "s_lshl_b32 s6, s6, 5\n"
      "s_cmp_lt_i32 s6, s5\n"
      "s_cbranch_scc1 .Lk_pc_74\n"
      "s_add_u32 s5, s5, s30\n"
      "s_xor_b32 s81, s83, 0x23400\n"
      "buffer_load_dwordx2 a[228:229], v96, s[88:91], s81 offen nt\n"
      "s_waitcnt vmcnt(40)\n"
      "buffer_load_dwordx2 a[236:237], v96, s[88:91], s81 offen offset:128 nt\n"
      "s_waitcnt vmcnt(40)\n"
      "buffer_load_dwordx2 a[244:245], v96, s[88:91], s81 offen offset:512 nt\n"
      "s_waitcnt vmcnt(40)\n"
      "buffer_load_dwordx2 a[252:253], v96, s[88:91], s81 offen offset:640 nt\n"
      "s_waitcnt vmcnt(40)\n"
      "s_memrealtime s[6:7]\n"
      "s_waitcnt lgkmcnt(0)\n"
      "s_sub_u32 s6, s6, s4\n"
      "s_lshl_b32 s6, s6, 5\n"
      "s_sub_u32 s6, s5, s6\n"
      "s_cmp_lt_i32 s6, 640\n"
      "s_cbranch_scc1 .Lk_pc_75\n"
      "s_sleep 5\n"
      ".Lk_pc_75:\n"
      "s_memrealtime s[6:7]\n"
      "s_waitcnt lgkmcnt(0)\n"
      "s_sub_u32 s6, s6, s4\n"
      "s_lshl_b32 s6, s6, 5\n"
      "s_cmp_lt_i32 s6, s5\n"
      "s_cbranch_scc1 .Lk_pc_75\n"
      "s_add_u32 s5, s5, s30\n"
      "s_xor_b32 s81, s83, 0x25000\n"
      "buffer_load_dwordx2 a[194:195], v96, s[88:91], s81 offen nt\n"
      "s_waitcnt vmcnt(40)\n"
      "buffer_load_dwordx2 a[202:203], v96, s[88:91], s81 offen offset:128 nt\n"
      "s_waitcnt vmcnt(40)\n"
      "buffer_load_dwordx2 a[210:211], v96, s[88:91], s81 offen offset:512 nt\n"
      "s_waitcnt vmcnt(40)\n"
      "buffer_load_dwordx2 a[218:219], v96, s[88:91], s81 offen offset:640 nt\n"
      "s_waitcnt vmcnt(40)\n"
      "s_getpc_b64 s[84:85]\n"
      ".Lk_pc2:\n"
      "s_and_b32 s85, s85, 0xffff\n"
      "s_mov_b32 s86, .Lk_end-.Lk_pc2\n"
      "s_and_b32 s68, s57, 0x3e0\n"
      "s_lshl_b32 s68, s68, 5\n"
      "s_and_b32 s79, s57, 0x3\n"
      "s_lshl_b32 s79, s79, 8\n"
      "s_add_u32 s68, s68, s79\n"
      "v_lshl_add_u32 v64, v0, 2, s68\n"
      "buffer_load_dword v63, v64, s[84:87], 0 offen \n"
      "s_waitcnt vmcnt(27)\n"
      "v_smfmac_f32_16x16x64_bf16 v[20:23], v[12:15], a[128:135], v1 abid:1\n"
      "v_mov_b64_dpp v[18:19], v[86:87] row_newbcast:6 row_mask:0xf bank_mask:0xf\n"
      "v_smfmac_f32_16x16x64_bf16 v[24:27], v[12:15], a[136:143], v1 abid:1\n"
      "s_waitcnt lgkmcnt(0)\n"
      "s_barrier\n"
      "v_mov_b64_dpp v[12:13], v[86:87] row_newbcast:8 row_mask:0xf bank_mask:0xf\n"
      "s_waitcnt vmcnt(25)\n"
      "v_smfmac_f32_16x16x64_bf16 v[20:23], v[16:19], a[144:151], v1 abid:1\n"
      "ds_read_b128 v[40:43], v29 offset:16\n"
      "v_mov_b64_dpp v[14:15], v[86:87] row_newbcast:10 row_mask:0xf bank_mask:0xf\n"
      "v_smfmac_f32_16x16x64_bf16 v[24:27], v[16:19], a[152:159], v1 abid:1\n"
      "ds_read_b128 v[28:31], v29\n"
      "v_mov_b64_dpp v[16:17], v[86:87] row_newbcast:12 row_mask:0xf bank_mask:0xf\n"
      "s_waitcnt vmcnt(23)\n"
      "v_smfmac_f32_16x16x64_bf16 v[20:23], v[12:15], a[160:167], v1 abid:1\n"
      "v_mov_b64_dpp v[18:19], v[86:87] row_newbcast:14 row_mask:0xf bank_mask:0xf\n"
      "v_smfmac_f32_16x16x64_bf16 v[24:27], v[12:15], a[168:175], v1 abid:1\n"
      "v_mov_b64_dpp v[12:13], v[86:87] row_newbcast:1 row_mask:0xf bank_mask:0xf\n"
      "s_waitcnt vmcnt(21)\n"
      "v_smfmac_f32_16x16x64_bf16 v[20:23], v[16:19], a[176:183], v1 abid:1\n"
      "v_mov_b64_dpp v[14:15], v[86:87] row_newbcast:3 row_mask:0xf bank_mask:0xf\n"
      "v_smfmac_f32_16x16x64_bf16 v[24:27], v[16:19], a[184:191], v1 abid:1\n"
      "v_mov_b64_dpp v[16:17], v[86:87] row_newbcast:5 row_mask:0xf bank_mask:0xf\n"
      "s_memrealtime s[6:7]\n"
      "s_waitcnt lgkmcnt(0)\n"
      "s_sub_u32 s6, s6, s4\n"
      "s_lshl_b32 s6, s6, 5\n"
      "s_sub_u32 s6, s5, s6\n"
      "s_cmp_lt_i32 s6, 640\n"
      "s_cbranch_scc1 .Lk_pc_76\n"
      "s_sleep 5\n"
      ".Lk_pc_76:\n"
      "s_memrealtime s[6:7]\n"
      "s_waitcnt lgkmcnt(0)\n"
      "s_sub_u32 s6, s6, s4\n"
      "s_lshl_b32 s6, s6, 5\n"
      "s_cmp_lt_i32 s6, s5\n"
      "s_cbranch_scc1 .Lk_pc_76\n"
      "s_add_u32 s5, s5, s30\n"
      "s_xor_b32 s81, s83, 0x25400\n"
      "buffer_load_dwordx2 a[226:227], v96, s[88:91], s81 offen nt\n"
      "buffer_load_dwordx2 a[234:235], v96, s[88:91], s81 offen offset:128 nt\n"
      "buffer_load_dwordx2 a[242:243], v96, s[88:91], s81 offen offset:512 nt\n"
      "buffer_load_dwordx2 a[250:251], v96, s[88:91], s81 offen offset:640 nt\n"
      "s_memrealtime s[6:7]\n"
      "s_waitcnt lgkmcnt(0)\n"
      "s_sub_u32 s6, s6, s4\n"
      "s_lshl_b32 s6, s6, 5\n"
      "s_sub_u32 s6, s5, s6\n"
      "s_cmp_lt_i32 s6, 640\n"
      "s_cbranch_scc1 .Lk_pc_77\n"
      "s_sleep 5\n"
      ".Lk_pc_77:\n"
      "s_memrealtime s[6:7]\n"
      "s_waitcnt lgkmcnt(0)\n"
      "s_sub_u32 s6, s6, s4\n"
      "s_lshl_b32 s6, s6, 5\n"
      "s_cmp_lt_i32 s6, s5\n"
      "s_cbranch_scc1 .Lk_pc_77\n"
      "s_add_u32 s5, s5, s30\n"
      "s_xor_b32 s81, s83, 0x27000\n"
      "buffer_load_dwordx2 a[198:199], v96, s[88:91], s81 offen nt\n"
      "buffer_load_dwordx2 a[206:207], v96, s[88:91], s81 offen offset:128 nt\n"
      "buffer_load_dwordx2 a[214:215], v96, s[88:91], s81 offen offset:512 nt\n"
      "buffer_load_dwordx2 a[222:223], v96, s[88:91], s81 offen offset:640 nt\n"
      "s_memrealtime s[6:7]\n"
      "s_waitcnt lgkmcnt(0)\n"
      "s_sub_u32 s6, s6, s4\n"
      "s_lshl_b32 s6, s6, 5\n"
      "s_sub_u32 s6, s5, s6\n"
      "s_cmp_lt_i32 s6, 640\n"
      "s_cbranch_scc1 .Lk_pc_78\n"
      "s_sleep 5\n"
      ".Lk_pc_78:\n"
      "s_memrealtime s[6:7]\n"
      "s_waitcnt lgkmcnt(0)\n"
      "s_sub_u32 s6, s6, s4\n"
      "s_lshl_b32 s6, s6, 5\n"
      "s_cmp_lt_i32 s6, s5\n"
      "s_cbranch_scc1 .Lk_pc_78\n"
      "s_add_u32 s5, s5, s30\n"
      "s_xor_b32 s81, s83, 0x27400\n"
      "buffer_load_dwordx2 a[230:231], v96, s[88:91], s81 offen nt\n"
      "buffer_load_dwordx2 a[238:239], v96, s[88:91], s81 offen offset:128 nt\n"
      "buffer_load_dwordx2 a[246:247], v96, s[88:91], s81 offen offset:512 nt\n"
      "buffer_load_dwordx2 a[254:255], v96, s[88:91], s81 offen offset:640 nt\n"
      "s_sub_u32 s5, s5, s30\n"
      "s_lshr_b32 s18, s5, 5\n"
      "s_add_u32 s18, s18, s4\n"
      "s_add_u32 s19, s18, 114\n"
      "s_mov_b32 s22, s18\n"
      "s_add_u32 s5, s5, 1600\n"
      "s_waitcnt vmcnt(6)\n"
      "v_smfmac_f32_16x16x64_bf16 v[20:23], v[12:15], a[192:199], v1 abid:1\n"
      "v_mov_b64_dpp v[18:19], v[86:87] row_newbcast:7 row_mask:0xf bank_mask:0xf\n"
      "v_smfmac_f32_16x16x64_bf16 v[24:27], v[12:15], a[200:207], v1 abid:1\n"
      "v_mov_b64_dpp v[12:13], v[86:87] row_newbcast:9 row_mask:0xf bank_mask:0xf\n"
      "s_waitcnt vmcnt(4)\n"
      "v_smfmac_f32_16x16x64_bf16 v[20:23], v[16:19], a[208:215], v1 abid:1\n"
      "v_mov_b64_dpp v[14:15], v[86:87] row_newbcast:11 row_mask:0xf bank_mask:0xf\n"
      "v_smfmac_f32_16x16x64_bf16 v[24:27], v[16:19], a[216:223], v1 abid:1\n"
      "v_mov_b64_dpp v[16:17], v[86:87] row_newbcast:13 row_mask:0xf bank_mask:0xf\n"
      "s_memrealtime s[6:7]\n"
      "s_waitcnt lgkmcnt(0)\n"
      "s_sub_u32 s6, s6, s4\n"
      "s_lshl_b32 s6, s6, 5\n"
      "s_sub_u32 s6, s5, s6\n"
      "s_cmp_lt_i32 s6, 640\n"
      "s_cbranch_scc1 .Lk_pc_80\n"
      "s_sleep 5\n"
      ".Lk_pc_80:\n"
      "s_memrealtime s[6:7]\n"
      "s_waitcnt lgkmcnt(0)\n"
      "s_sub_u32 s6, s6, s4\n"
      "s_lshl_b32 s6, s6, 5\n"
      "s_cmp_lt_i32 s6, s5\n"
      "s_cbranch_scc1 .Lk_pc_80\n"
      ".Lk_cl_79:\n"
      "s_cmp_lg_u32 s61, 1\n"
      "s_cbranch_scc1 .Lk_tg_87\n"
      "s_add_u32 m0, s58, 0\n"
      "s_mov_b32 s6, 0\n"
      "buffer_load_dwordx4 v97, s[52:55], s6 offen nt lds\n"
      "s_add_u32 m0, s58, 1024\n"
      "s_mov_b32 s6, 5504\n"
      "buffer_load_dwordx4 v97, s[52:55], s6 offen nt lds\n"
      "s_add_u32 m0, s58, 2048\n"
      "s_mov_b32 s6, 44032\n"
      "buffer_load_dwordx4 v97, s[52:55], s6 offen nt lds\n"
      "s_add_u32 m0, s58, 3072\n"
      "s_mov_b32 s6, 49536\n"
      "buffer_load_dwordx4 v97, s[52:55], s6 offen nt lds\n"
      "s_branch .Lk_tp_85\n"
      ".Lk_tg_87:\n"
      "s_cmp_lg_u32 s61, 2\n"
      "s_cbranch_scc1 .Lk_tg_88\n"
      "s_add_u32 s6, s82, 0\n"
      "buffer_store_dwordx4 v[70:73], v66, s[40:43], s6 offen sc1 nt\n"
      "s_add_u32 s6, s82, 65536\n"
      "buffer_store_dwordx4 v[70:73], v66, s[40:43], s6 offen sc1 nt\n"
      "s_add_u32 s6, s82, 131072\n"
      "buffer_store_dwordx4 v[70:73], v67, s[40:43], s6 offen sc1 nt\n"
      "buffer_store_dwordx4 v[70:73], v68, s[40:43], 0 offen sc1 nt\n"
      "buffer_store_dwordx4 v[70:73], v68, s[40:43], 0 offen offset:1024 sc1 nt\n"
      "s_branch .Lk_tp_86\n"
      ".Lk_tg_88:\n"
      ".Lk_tp_81:\n"
      "buffer_load_dword v63, v62, s[40:43], 0 offen \n"
      ".Lk_tp_82:\n"
      "buffer_load_dword v63, v62, s[40:43], 0 offen \n"
      ".Lk_tp_83:\n"
      "buffer_load_dword v63, v62, s[40:43], 0 offen \n"
      ".Lk_tp_84:\n"
      "buffer_load_dword v63, v62, s[40:43], 0 offen \n"
      ".Lk_tp_85:\n"
      "buffer_load_dword v63, v62, s[40:43], 0 offen \n"
      ".Lk_tp_86:\n"
      "s_waitcnt vmcnt(7)\n"
      "v_smfmac_f32_16x16x64_bf16 v[20:23], v[12:15], a[224:231], v1 abid:1\n"
      "v_mov_b64_dpp v[18:19], v[86:87] row_newbcast:15 row_mask:0xf bank_mask:0xf\n"
      "v_smfmac_f32_16x16x64_bf16 v[24:27], v[12:15], a[232:239], v1 abid:1\n"
      "s_waitcnt vmcnt(5)\n"
      "v_smfmac_f32_16x16x64_bf16 v[20:23], v[16:19], a[240:247], v1 abid:1\n"
      "v_smfmac_f32_16x16x64_bf16 v[24:27], v[16:19], a[248:255], v1 abid:1\n"
      "s_branch .Lk_tail\n"
      ".Lk_fill:\n"
      ".fill ((32320 - (.Lk_fill - .Lk_start)) & 4095) / 4, 4, 0xbf800000\n"
      ".Lk_wret:\n"
      "s_setpc_b64 s[6:7]\n"
      ".Lk_idle:\n"
      "s_waitcnt vmcnt(0)\n"
      ".Lk_next:\n"
      "s_cmp_lg_u32 s62, 0\n"
      "s_cbranch_scc1 .Lk_exit\n"
      "s_mov_b32 s68, 106496\n"
      "s_and_b32 s69, s57, 0x3\n"
      "s_lshl_b32 s69, s69, 5\n"
      "v_bfe_u32 v11, v0, 5, 1\n"
      "v_lshl_add_u32 v49, v11, 12, s68\n"
      "v_bfe_u32 v11, v0, 3, 1\n"
      "v_lshl_add_u32 v49, v11, 9, v49\n"
      "v_bfe_u32 v11, v0, 3, 1\n"
      "v_lshl_add_u32 v49, v11, 6, v49\n"
      "v_bfe_u32 v11, v0, 4, 1\n"
      "v_lshl_add_u32 v49, v11, 8, v49\n"
      "v_bfe_u32 v11, v0, 4, 1\n"
      "v_lshl_add_u32 v49, v11, 5, v49\n"
      "v_bfe_u32 v11, v0, 2, 1\n"
      "v_lshl_add_u32 v49, v11, 7, v49\n"
      "v_bfe_u32 v11, v0, 0, 2\n"
      "v_lshl_add_u32 v49, v11, 3, v49\n"
      "v_xor_b32 v49, s69, v49\n"
      "ds_read_b64_tr_b16 v[28:29], v49 offset:0\n"
      "ds_read_b64_tr_b16 v[30:31], v49 offset:1024\n"
      "ds_read_b64_tr_b16 v[32:33], v49 offset:8192\n"
      "ds_read_b64_tr_b16 v[34:35], v49 offset:9216\n"
      "ds_read_b64_tr_b16 v[36:37], v49 offset:2048\n"
      "ds_read_b64_tr_b16 v[38:39], v49 offset:3072\n"
      "ds_read_b64_tr_b16 v[40:41], v49 offset:10240\n"
      "ds_read_b64_tr_b16 v[42:43], v49 offset:11264\n"
      "s_and_b32 s68, s57, 0xc0\n"
      "s_lshl_b32 s68, s68, 2\n"
      "s_and_b32 s79, s57, 0x1\n"
      "s_lshl_b32 s79, s79, 11\n"
      "s_add_u32 s68, s68, s79\n"
      "v_bfe_u32 v11, v0, 2, 1\n"
      "v_lshl_add_u32 v46, v11, 10, s68\n"
      "v_bfe_u32 v11, v0, 4, 2\n"
      "v_lshl_add_u32 v46, v11, 6, v46\n"
      "v_bfe_u32 v11, v0, 0, 2\n"
      "v_lshl_add_u32 v46, v11, 4, v46\n"
      "s_and_b32 s68, s57, 0x80\n"
      "s_lshl_b32 s68, s68, 3\n"
      "s_and_b32 s79, s57, 0x1\n"
      "s_lshl_b32 s79, s79, 11\n"
      "s_add_u32 s68, s68, s79\n"
      "s_add_u32 s68, s68, 0x1000\n"
      "v_lshl_add_u32 v47, v0, 4, s68\n"
      "s_and_b32 s68, s57, 0x1c\n"
      "s_lshl_b32 s68, s68, 13\n"
      "s_and_b32 s79, s57, 0x200\n"
      "s_lshl_b32 s79, s79, 5\n"
      "s_add_u32 s68, s68, s79\n"
      "s_and_b32 s79, s57, 0x100\n"
      "s_lshl_b32 s79, s79, 4\n"
      "s_add_u32 s68, s68, s79\n"
      "s_and_b32 s79, s57, 0x20\n"
      "s_lshl_b32 s79, s79, 5\n"
      "s_add_u32 s68, s68, s79\n"
      "s_and_b32 s79, s57, 0x3\n"
      "s_lshl_b32 s79, s79, 8\n"
      "s_add_u32 s68, s68, s79\n"
      "s_and_b32 s79, s57, 0x40\n"
      "s_lshl_b32 s79, s79, 1\n"
      "s_add_u32 s68, s68, s79\n"
      "v_bfe_u32 v11, v0, 0, 4\n"
      "v_lshl_add_u32 v48, v11, 2, s68\n"
      "v_bfe_u32 v11, v0, 4, 1\n"
      "v_lshl_add_u32 v48, v11, 11, v48\n"
      "v_bfe_u32 v11, v0, 5, 1\n"
      "v_lshl_add_u32 v48, v11, 13, v48\n"
      "s_and_b32 s68, s57, 0x80\n"
      "s_lshr_b32 s68, s68, 1\n"
      "s_and_b32 s79, s57, 0x80\n"
      "s_lshr_b32 s79, s79, 4\n"
      "s_add_u32 s68, s68, s79\n"
      "v_xor_b32 v48, s68, v48\n"
      "s_mov_b32 s78, 0xffffffff\n"
      "s_mov_b32 s79, 0x0\n"
      "v_mov_b32 v11, 0x40000000\n"
      "v_cndmask_b32 v48, v11, v48, s[78:79]\n"
      "s_and_b32 s68, s57, 0x1c\n"
      "s_lshl_b32 s68, s68, 13\n"
      "s_and_b32 s79, s57, 0x200\n"
      "s_lshl_b32 s79, s79, 5\n"
      "s_add_u32 s68, s68, s79\n"
      "s_and_b32 s79, s57, 0x100\n"
      "s_lshl_b32 s79, s79, 4\n"
      "s_add_u32 s68, s68, s79\n"
      "s_and_b32 s79, s57, 0x80\n"
      "s_lshl_b32 s79, s79, 4\n"
      "s_add_u32 s68, s68, s79\n"
      "v_bfe_u32 v11, v0, 3, 3\n"
      "v_lshl_add_u32 v96, v11, 8, s68\n"
      "v_bfe_u32 v11, v0, 0, 1\n"
      "v_lshl_add_u32 v96, v11, 6, v96\n"
      "v_bfe_u32 v11, v0, 1, 1\n"
      "v_lshl_add_u32 v96, v11, 5, v96\n"
      "v_bfe_u32 v11, v0, 2, 1\n"
      "v_lshl_add_u32 v96, v11, 4, v96\n"
      "s_and_b32 s68, s57, 0x1c\n"
      "s_lshl_b32 s68, s68, 5\n"
      "s_add_u32 s68, s68, 0x174400\n"
      "v_mov_b32 v12, s68\n"
      "s_add_u32 s19, s18, 296\n"
      "s_branch .Lk_nb_94\n"
      ".Lk_na_93:\n"
      "s_sleep 4\n"
      ".Lk_nb_94:\n"
      "s_memrealtime s[6:7]\n"
      "s_waitcnt lgkmcnt(0)\n"
      "s_sub_u32 s23, s6, s19\n"
      "s_cmp_lt_i32 s23, -24\n"
      "s_cbranch_scc1 .Lk_na_93\n"
      ".Lk_un_89:\n"
      "s_memrealtime s[84:85]\n"
      "s_waitcnt lgkmcnt(2)\n"
      "s_sub_u32 s23, s6, s19\n"
      "s_cmp_lt_i32 s23, -3\n"
      "s_cbranch_scc0 .Lk_uo_90\n"
      "s_nop 4\n"
      "s_memrealtime s[6:7]\n"
      "s_waitcnt lgkmcnt(2)\n"
      "s_sub_u32 s23, s84, s19\n"
      "s_cmp_lt_i32 s23, -3\n"
      "s_cbranch_scc1 .Lk_un_89\n"
      "s_nop 4\n"
      ".Lk_uo_90:\n"
      ".Lk_uf_91:\n"
      "s_addk_i32 s23, 1\n"
      "s_cmp_lt_i32 s23, 1\n"
      "s_cbranch_scc1 .Lk_uf_91\n"
      ".Lk_tick:\n"
      "buffer_load_dword v13, v12, s[40:43], s63 offen sc1\n"
      "s_sub_u32 s66, s66, 1\n"
      "s_cselect_b32 s67, -1, s67\n"
      "s_waitcnt vmcnt(0)\n"
      "v_cmp_lt_u32 vcc, s67, v13\n"
      "s_cbranch_vccnz .Lk_tick_retry\n"
      "v_readfirstlane_b32 s68, v13\n"
      "s_nop 0\n"
      "s_sub_u32 s68, s68, s22\n"
      "s_bfe_i32 s68, s68, 0x1f0000\n"
      "s_min_i32 s68, s68, 4096\n"
      "s_add_u32 s18, s22, s68\n"
      ".Lk_s5:\n"
      "s_add_u32 s68, s63, 1507328\n"
      "s_add_u32 s44, s40, s68\n"
      "s_addc_u32 s45, s41, 0\n"
      "s_mov_b32 s46, 8192\n"
      "s_add_u32 s19, s18, 339\n"
      "s_branch .Lk_nb_100\n"
      ".Lk_na_99:\n"
      "s_sleep 4\n"
      ".Lk_nb_100:\n"
      "s_memrealtime s[6:7]\n"
      "s_waitcnt lgkmcnt(0)\n"
      "s_sub_u32 s23, s6, s19\n"
      "s_cmp_lt_i32 s23, -24\n"
      "s_cbranch_scc1 .Lk_na_99\n"
      ".Lk_un_95:\n"
      "s_memrealtime s[84:85]\n"
      "s_waitcnt lgkmcnt(2)\n"
      "s_sub_u32 s23, s6, s19\n"
      "s_cmp_lt_i32 s23, -3\n"
      "s_cbranch_scc0 .Lk_uo_96\n"
      "s_nop 4\n"
      "s_memrealtime s[6:7]\n"
      "s_waitcnt lgkmcnt(2)\n"
      "s_sub_u32 s23, s84, s19\n"
      "s_cmp_lt_i32 s23, -3\n"
      "s_cbranch_scc1 .Lk_un_95\n"
      "s_nop 4\n"
      ".Lk_uo_96:\n"
      ".Lk_uf_97:\n"
      "s_addk_i32 s23, 1\n"
      "s_cmp_lt_i32 s23, 1\n"
      "s_cbranch_scc1 .Lk_uf_97\n"
      "buffer_load_dwordx4 v[12:15], v46, s[44:47], 0 offen sc1\n"
      "s_waitcnt lgkmcnt(0)\n"
      "s_add_u32 s20, s19, s23\n"
      "s_cmp_gt_i32 s23, 8\n"
      "s_cselect_b32 s7, s23, 0\n"
      "s_add_u32 s18, s18, s7\n"
      "s_add_u32 s19, s18, 383\n"
      "ds_read_b128 v[60:63], v2 offset:0\n"
      "ds_read_b128 v[64:67], v2 offset:1024\n"
      "ds_read_b128 v[68:71], v2 offset:2048\n"
      "ds_read_b128 v[72:75], v2 offset:3072\n"
      "ds_read_b128 v[76:79], v2 offset:4096\n"
      "ds_read_b128 v[80:83], v2 offset:5120\n"
      "s_mov_b32 s62, 622592\n"
      "s_mov_b32 s80, 1245184\n"
      "s_add_u32 s81, s80, s63\n"
      "s_add_u32 s74, s62, s63\n"
      "s_mov_b32 s12, s74\n"
      "s_nop 15\n"
      "buffer_load_dwordx4 v[24:27], v47, s[44:47], 0 offen sc1\n"
      ".Lk_x_wait:\n"
      "v_mov_b64 v[16:17], 0\n"
      "v_mov_b64 v[18:19], 0\n"
      "v_mov_b64 v[20:21], 0\n"
      "v_mov_b64 v[22:23], 0\n"
      "s_waitcnt vmcnt(1)\n"
      "v_smfmac_f32_16x16x64_bf16 v[16:19], v[12:15], v[28:35], v1\n"
      "v_smfmac_f32_16x16x64_bf16 v[20:23], v[12:15], v[36:43], v1\n"
      "v_max3_u32 v11, v12, v13, v14\n"
      "v_max_u32 v11, v11, v15\n"
      "v_cmp_lt_u32 vcc, s67, v11\n"
      "s_cbranch_vccnz .Lk_x_retry\n"
      "s_nop 2\n"
      "v_add_f32 v16, v16, v17\n"
      "v_add_f32 v22, v22, v23\n"
      "v_add_f32 v16, v16, v22\n"
      "buffer_store_dword v16, v48, s[40:43], s81 offen sc1\n"
      "s_branch .Lk_nb_106\n"
      ".Lk_na_105:\n"
      "s_sleep 4\n"
      ".Lk_nb_106:\n"
      "s_memrealtime s[6:7]\n"
      "s_waitcnt lgkmcnt(0)\n"
      "s_sub_u32 s23, s6, s19\n"
      "s_cmp_lt_i32 s23, -24\n"
      "s_cbranch_scc1 .Lk_na_105\n"
      ".Lk_un_101:\n"
      "s_memrealtime s[84:85]\n"
      "s_waitcnt lgkmcnt(2)\n"
      "s_sub_u32 s23, s6, s19\n"
      "s_cmp_lt_i32 s23, -3\n"
      "s_cbranch_scc0 .Lk_uo_102\n"
      "s_nop 4\n"
      "s_memrealtime s[6:7]\n"
      "s_waitcnt lgkmcnt(2)\n"
      "s_sub_u32 s23, s84, s19\n"
      "s_cmp_lt_i32 s23, -3\n"
      "s_cbranch_scc1 .Lk_un_101\n"
      "s_nop 4\n"
      ".Lk_uo_102:\n"
      ".Lk_uf_103:\n"
      "s_addk_i32 s23, 1\n"
      "s_cmp_lt_i32 s23, 1\n"
      "s_cbranch_scc1 .Lk_uf_103\n"
      "buffer_load_dwordx4 v[88:91], v96, s[40:43], s81 offen sc1\n"
      "buffer_load_dwordx4 v[92:95], v96, s[40:43], s81 offen offset:128 sc1\n"
      "s_waitcnt lgkmcnt(0)\n"
      "s_add_u32 s20, s19, s23\n"
      "s_cmp_gt_i32 s23, 8\n"
      "s_cselect_b32 s7, s23, 0\n"
      "s_add_u32 s18, s18, s7\n"
      "s_add_u32 s19, s18, 507\n"
      "v_mov_b64 v[12:13], 0\n"
      "v_mov_b64 v[14:15], 0\n"
      "v_mov_b64 v[16:17], 0\n"
      "v_mov_b64 v[18:19], 0\n"
      "v_mov_b64 v[20:21], 0\n"
      "v_mov_b64 v[22:23], 0\n"
      "v_mov_b64 v[28:29], 0\n"
      "v_mov_b64 v[30:31], 0\n"
      "v_mov_b64 v[32:33], 0\n"
      "v_mov_b64 v[34:35], 0\n"
      "v_mov_b64 v[36:37], 0\n"
      "v_mov_b64 v[38:39], 0\n"
      "v_mov_b64 v[40:41], 0\n"
      "v_mov_b64 v[42:43], 0\n"
      "v_mov_b64 v[44:45], 0\n"
      "v_mov_b64 v[48:49], 0\n"
      "v_mov_b64 v[52:53], 0\n"
      "v_mov_b64 v[54:55], 0\n"
      "v_mov_b64 v[56:57], 0\n"
      "v_mov_b64 v[58:59], 0\n"
      ".Lk_xs_chk:\n"
      "s_waitcnt vmcnt(3)\n"
      "v_max3_u32 v11, v24, v25, v26\n"
      "v_max_u32 v11, v11, v27\n"
      "v_cmp_lt_u32 vcc, s67, v11\n"
      "s_cbranch_vccnz .Lk_xs_retry\n"
      "s_mov_b32 s68, 0x3f803f80\n"
      "v_dot2_f32_bf16 v50, v24, s68, 0\n"
      "v_dot2_f32_bf16 v51, v24, v24, 0\n"
      "v_dot2_f32_bf16 v50, v25, s68, v50\n"
      "v_dot2_f32_bf16 v51, v25, v25, v51\n"
      "v_dot2_f32_bf16 v50, v26, s68, v50\n"
      "v_dot2_f32_bf16 v51, v26, v26, v51\n"
      "v_dot2_f32_bf16 v50, v27, s68, v50\n"
      "v_dot2_f32_bf16 v51, v27, v27, v51\n"
      "s_nop 2\n"
      "v_permlane32_swap_b32 v50, v51\n"
      "v_add_f32 v50, v50, v51\n"
      "s_nop 1\n"
      "v_add_f32_dpp v50, v50, v50 row_ror:8 row_mask:0xf bank_mask:0xf\n"
      "s_nop 1\n"
      "v_add_f32_dpp v50, v50, v50 row_ror:4 row_mask:0xf bank_mask:0xf\n"
      "s_nop 1\n"
      "v_add_f32_dpp v50, v50, v50 row_ror:2 row_mask:0xf bank_mask:0xf\n"
      "s_nop 1\n"
      "v_add_f32_dpp v50, v50, v50 row_ror:1 row_mask:0xf bank_mask:0xf\n"
      "s_nop 1\n"
      "v_add_f32_dpp v50, v50, v50 row_bcast:15 row_mask:0xa bank_mask:0xf\n"
      "s_nop 0\n"
      "v_readlane_b32 s68, v50, 31\n"
      "v_readlane_b32 s69, v50, 63\n"
      "v_mov_b32 v51, 0xbb000000\n"
      "v_mul_f32 v50, s68, v51\n"
      "v_mul_f32 v51, s69, v51\n"
      "v_fma_f32 v51, v50, v50, v51\n"
      "v_sub_f32 v51, 0x3727c5ac, v51\n"
      "v_max_f32 v51, 0x3727c5ac, v51\n"
      "v_rsq_f32 v51, v51\n"
      "s_nop 0\n"
      "v_mul_f32 v10, 0x3e0293ee, v51\n"
      "v_readfirstlane_b32 s10, v50\n"
      "v_mov_b64 v[24:25], 0\n"
      "v_mov_b64 v[26:27], 0\n"
      "v_mov_b64 v[46:47], 0\n"
      "v_mov_b64 v[50:51], 0\n"
      ".Lk_q_wait:\n"
      "v_mul_f32 v84, s10, v4\n"
      "v_mul_f32 v85, s10, v5\n"
      "v_mul_f32 v86, s10, v6\n"
      "v_mul_f32 v87, s10, v7\n"
      "s_waitcnt vmcnt(1)\n"
      "v_max3_u32 v11, v88, v89, v90\n"
      "v_max_u32 v11, v11, v91\n"
      "v_pk_add_f32 v[84:85], v[84:85], v[88:89]\n"
      "v_pk_add_f32 v[86:87], v[86:87], v[90:91]\n"
      "s_waitcnt vmcnt(0)\n"
      "v_max3_u32 v11, v11, v92, v93\n"
      "v_max3_u32 v11, v11, v94, v95\n"
      "v_cmp_lt_u32 vcc, s67, v11\n"
      "s_cbranch_vccnz .Lk_q_retry\n"
      "v_pk_add_f32 v[90:91], v[86:87], v[94:95]\n"
      "v_pk_add_f32 v[88:89], v[84:85], v[92:93]\n"
      "s_nop 1\n"
      "v_add_f32_dpp v88, v90, v88 quad_perm:[1,0,3,2] row_mask:0xf bank_mask:0xf\n"
      "v_add_f32_dpp v89, v91, v89 quad_perm:[1,0,3,2] row_mask:0xf bank_mask:0xf\n"
      "v_cvt_pk_bf16_f32 v88, v88, v89\n"
      "s_nop 1\n"
      "v_mov_b32_dpp v12, v88 quad_perm:[0,1,2,3] row_mask:0xf bank_mask:0x1\n"
      "v_mov_b32_dpp v13, v88 row_ror:12 row_mask:0xf bank_mask:0x1\n"
      "v_mov_b32_dpp v14, v88 row_ror:8 row_mask:0xf bank_mask:0x1\n"
      "v_mov_b32_dpp v15, v88 row_ror:4 row_mask:0xf bank_mask:0x1\n"
      "s_nop 1\n"
      "v_smfmac_f32_16x16x64_bf16 v[28:31], v[12:15], v[104:111], v1\n"
      "v_mov_b32_dpp v16, v88 row_ror:4 row_mask:0xf bank_mask:0x2\n"
      "v_mov_b32_dpp v17, v88 quad_perm:[0,1,2,3] row_mask:0xf bank_mask:0x2\n"
      "v_smfmac_f32_16x16x64_bf16 v[32:35], v[12:15], v[60:67], v1\n"
      "ds_read_b128 v[60:63], v2 offset:6144\n"
      "v_mov_b32_dpp v18, v88 row_ror:12 row_mask:0xf bank_mask:0x2\n"
      "v_smfmac_f32_16x16x64_bf16 v[36:39], v[12:15], v[144:151], v1\n"
      "ds_read_b128 v[64:67], v2 offset:7168\n"
      "v_mov_b32_dpp v19, v88 row_ror:8 row_mask:0xf bank_mask:0x2\n"
      "v_smfmac_f32_16x16x64_bf16 v[40:43], v[12:15], v[68:75], v1\n"
      "ds_read_b128 v[68:71], v2 offset:8192\n"
      "v_smfmac_f32_16x16x64_bf16 v[28:31], v[16:19], v[112:119], v1\n"
      "ds_read_b128 v[72:75], v2 offset:9216\n"
      "v_mov_b32_dpp v20, v88 row_ror:8 row_mask:0xf bank_mask:0x4\n"
      "v_mov_b32_dpp v21, v88 row_ror:4 row_mask:0xf bank_mask:0x4\n"
      "v_smfmac_f32_16x16x64_bf16 v[32:35], v[16:19], v[128:135], v1\n"
      "v_mov_b32_dpp v22, v88 quad_perm:[0,1,2,3] row_mask:0xf bank_mask:0x4\n"
      "v_smfmac_f32_16x16x64_bf16 v[36:39], v[16:19], v[76:83], v1\n"
      "ds_read_b128 v[76:79], v2 offset:10240\n"
      "v_mov_b32_dpp v23, v88 row_ror:12 row_mask:0xf bank_mask:0x4\n"
      "v_smfmac_f32_16x16x64_bf16 v[40:43], v[16:19], v[168:175], v1\n"
      "ds_read_b128 v[80:83], v2 offset:11264\n"
      "s_waitcnt lgkmcnt(4)\n"
      "v_smfmac_f32_16x16x64_bf16 v[28:31], v[20:23], v[60:67], v1\n"
      "ds_read_b128 v[60:63], v2 offset:12288\n"
      "v_mov_b32_dpp v24, v88 row_ror:12 row_mask:0xf bank_mask:0x8\n"
      "v_mov_b32_dpp v25, v88 row_ror:8 row_mask:0xf bank_mask:0x8\n"
      "v_smfmac_f32_16x16x64_bf16 v[32:35], v[20:23], v[136:143], v1\n"
      "ds_read_b128 v[64:67], v2 offset:13312\n"
      "v_mov_b32_dpp v26, v88 row_ror:4 row_mask:0xf bank_mask:0x8\n"
      "v_smfmac_f32_16x16x64_bf16 v[36:39], v[20:23], v[152:159], v1\n"
      "s_waitcnt lgkmcnt(4)\n"
      "v_mov_b32_dpp v27, v88 quad_perm:[0,1,2,3] row_mask:0xf bank_mask:0x8\n"
      "v_smfmac_f32_16x16x64_bf16 v[40:43], v[20:23], v[68:75], v1\n"
      "ds_read_b128 v[68:71], v2 offset:14336\n"
      "v_smfmac_f32_16x16x64_bf16 v[28:31], v[24:27], v[120:127], v1\n"
      "ds_read_b128 v[72:75], v2 offset:15360\n"
      "s_waitcnt lgkmcnt(4)\n"
      "v_smfmac_f32_16x16x64_bf16 v[32:35], v[24:27], v[76:83], v1\n"
      "ds_read_b128 v[76:79], v2 offset:16384\n"
      "s_nop 0\n"
      "v_smfmac_f32_16x16x64_bf16 v[36:39], v[24:27], v[160:167], v1\n"
      "ds_read_b128 v[80:83], v2 offset:17408\n"
      "s_nop 0\n"
      "v_smfmac_f32_16x16x64_bf16 v[40:43], v[24:27], v[176:183], v1\n"
      "s_waitcnt lgkmcnt(4)\n"
      "s_nop 0\n"
      "v_smfmac_f32_16x16x64_bf16 v[44:47], v[12:15], v[60:67], v1\n"
      "ds_read_b128 v[60:63], v2 offset:18432\n"
      "s_nop 0\n"
      "v_add_f32 v30, v30, v31\n"
      "v_smfmac_f32_16x16x64_bf16 v[44:47], v[16:19], v[184:191], v1\n"
      "ds_read_b128 v[64:67], v2 offset:19456\n"
      "s_waitcnt lgkmcnt(4)\n"
      "v_add_f32 v28, v28, v29\n"
      "v_smfmac_f32_16x16x64_bf16 v[44:47], v[20:23], v[68:75], v1\n"
      "ds_read_b128 v[68:71], v2 offset:20480\n"
      "s_nop 0\n"
      "v_add_f32 v34, v34, v35\n"
      "v_smfmac_f32_16x16x64_bf16 v[44:47], v[24:27], v[192:199], v1\n"
      "ds_read_b128 v[72:75], v2 offset:21504\n"
      "s_nop 0\n"
      "v_add_f32_dpp v29, v30, v28 quad_perm:[1,0,3,2] row_mask:0xf bank_mask:0xf\n"
      "v_smfmac_f32_16x16x64_bf16 v[52:55], v[12:15], v[216:223], v1\n"
      "s_waitcnt lgkmcnt(4)\n"
      "s_nop 0\n"
      "v_add_f32 v32, v32, v33\n"
      "v_smfmac_f32_16x16x64_bf16 v[52:55], v[16:19], v[76:83], v1\n"
      "ds_read_b128 v[76:79], v2 offset:22528\n"
      "s_nop 0\n"
      "v_add_f32 v36, v36, v37\n"
      "v_smfmac_f32_16x16x64_bf16 v[52:55], v[20:23], v[224:231], v1\n"
      "ds_read_b128 v[80:83], v2 offset:23552\n"
      "s_waitcnt lgkmcnt(4)\n"
      "v_add_f32_dpp v33, v34, v32 quad_perm:[1,0,3,2] row_mask:0xf bank_mask:0xf\n"
      "v_smfmac_f32_16x16x64_bf16 v[52:55], v[24:27], v[60:67], v1\n"
      "ds_read_b128 v[60:63], v2 offset:24576\n"
      "s_nop 0\n"
      "v_add_f32 v38, v38, v39\n"
      "v_smfmac_f32_16x16x64_bf16 v[48:51], v[12:15], v[200:207], v1\n"
      "ds_read_b128 v[64:67], v2 offset:25600\n"
      "s_waitcnt lgkmcnt(4)\n"
      "v_add_f32 v40, v40, v41\n"
      "v_smfmac_f32_16x16x64_bf16 v[48:51], v[16:19], v[68:75], v1\n"
      "s_nop 1\n"
      "v_add_f32_dpp v37, v36, v38 quad_perm:[1,0,3,2] row_mask:0xf bank_mask:0xf\n"
      "v_smfmac_f32_16x16x64_bf16 v[48:51], v[20:23], v[208:215], v1\n"
      "s_waitcnt lgkmcnt(2)\n"
      "s_nop 0\n"
      "v_add_f32 v42, v42, v43\n"
      "v_smfmac_f32_16x16x64_bf16 v[48:51], v[24:27], v[76:83], v1\n"
      "s_nop 1\n"
      "v_cndmask_b32 v84, v29, v37, s[64:65]\n"
      "v_smfmac_f32_16x16x64_bf16 v[56:59], v[12:15], v[232:239], v1\n"
      "s_waitcnt lgkmcnt(0)\n"
      "s_nop 0\n"
      "v_add_f32_dpp v41, v40, v42 quad_perm:[1,0,3,2] row_mask:0xf bank_mask:0xf\n"
      "v_smfmac_f32_16x16x64_bf16 v[56:59], v[16:19], v[60:67], v1\n"
      "s_nop 1\n"
      "v_add_f32 v46, v46, v47\n"
      "v_smfmac_f32_16x16x64_bf16 v[56:59], v[20:23], v[240:247], v1\n"
      "s_nop 1\n"
      "v_cndmask_b32 v85, v33, v41, s[64:65]\n"
      "v_smfmac_f32_16x16x64_bf16 v[56:59], v[24:27], v[248:255], v1\n"
      "s_nop 1\n"
      "v_add_f32 v44, v44, v45\n"
      "v_add_f32 v52, v52, v53\n"
      "v_add_f32 v54, v54, v55\n"
      "v_add_f32_dpp v45, v46, v44 quad_perm:[1,0,3,2] row_mask:0xf bank_mask:0xf\n"
      "s_nop 0\n"
      "v_add_f32_dpp v53, v52, v54 quad_perm:[1,0,3,2] row_mask:0xf bank_mask:0xf\n"
      "v_add_f32 v50, v50, v51\n"
      "v_add_f32 v48, v48, v49\n"
      "v_cndmask_b32 v86, v45, v53, s[64:65]\n"
      "s_nop 0\n"
      "v_add_f32_dpp v49, v50, v48 quad_perm:[1,0,3,2] row_mask:0xf bank_mask:0xf\n"
      "v_add_f32 v56, v56, v57\n"
      "v_add_f32 v58, v58, v59\n"
      "v_max3_f32 v88, v84, v85, v86\n"
      "s_nop 0\n"
      "v_add_f32_dpp v57, v56, v58 quad_perm:[1,0,3,2] row_mask:0xf bank_mask:0xf\n"
      "v_cndmask_b32 v87, v49, v57, s[64:65]\n"
      "v_max_f32 v88, v88, v87\n"
      "v_mul_f32 v88, v10, v88\n"
      "v_mov_b64 v[20:21], 0\n"
      "v_mov_b64 v[22:23], 0\n"
      "v_max_f32_dpp v88, v88, v88 row_ror:8 row_mask:0xf bank_mask:0xf\n"
      "v_mov_b64 v[24:25], 0\n"
      "v_mov_b64 v[26:27], 0\n"
      "v_max_f32_dpp v88, v88, v88 row_ror:4 row_mask:0xf bank_mask:0xf\n"
      "v_mul_f32 v84, v10, v84\n"
      "v_mul_f32 v85, v10, v85\n"
      "v_max_f32_dpp v88, v88, v88 row_ror:2 row_mask:0xf bank_mask:0xf\n"
      "v_mul_f32 v86, v10, v86\n"
      "v_mul_f32 v87, v10, v87\n"
      "v_max_f32_dpp v88, v88, v88 row_ror:1 row_mask:0xf bank_mask:0xf\n"
      "v_lshrrev_b32 v28, 4, v0\n"
      "v_mov_b32 v29, 162912\n"
      "v_max_f32_dpp v88, v88, v88 row_bcast:15 row_mask:0xa bank_mask:0xf\n"
      "v_lshl_add_u32 v28, v28, 5, s59\n"
      "s_nop 0\n"
      "v_max_f32_dpp v88, v88, v88 row_bcast:31 row_mask:0xc bank_mask:0xf\n"
      "s_nop 0\n"
      "v_readlane_b32 s10, v88, 63\n"
      "s_nop 1\n"
      "v_subrev_f32 v88, s10, v84\n"
      "v_subrev_f32 v89, s10, v85\n"
      "v_subrev_f32 v90, s10, v86\n"
      "v_subrev_f32 v91, s10, v87\n"
      "v_exp_f32 v88, v88\n"
      "v_exp_f32 v89, v89\n"
      "v_cvt_pk_bf16_f32 v84, v88, 0\n"
      "v_cvt_pk_bf16_f32 v85, v89, 0\n"
      "v_exp_f32 v90, v90\n"
      "v_exp_f32 v91, v91\n"
      "v_mov_b64_dpp v[12:13], v[84:85] row_newbcast:0 row_mask:0xf bank_mask:0xf\n"
      "v_mov_b64_dpp v[14:15], v[84:85] row_newbcast:2 row_mask:0xf bank_mask:0xf\n"
      "v_mov_b64_dpp v[16:17], v[84:85] row_newbcast:4 row_mask:0xf bank_mask:0xf\n"
      "s_nop 0\n"
      "v_smfmac_f32_16x16x64_bf16 v[20:23], v[12:15], a[0:7], v1 abid:1\n"
      "v_cvt_pk_bf16_f32 v86, v90, 0\n"
      "v_mov_b64_dpp v[18:19], v[84:85] row_newbcast:6 row_mask:0xf bank_mask:0xf\n"
      "v_smfmac_f32_16x16x64_bf16 v[24:27], v[12:15], a[8:15], v1 abid:1\n"
      "v_cvt_pk_bf16_f32 v87, v91, 0\n"
      "v_mov_b64_dpp v[12:13], v[84:85] row_newbcast:8 row_mask:0xf bank_mask:0xf\n"
      "v_smfmac_f32_16x16x64_bf16 v[20:23], v[16:19], a[16:23], v1 abid:1\n"
      "v_mov_b32 v92, s10\n"
      "v_mov_b64_dpp v[14:15], v[84:85] row_newbcast:10 row_mask:0xf bank_mask:0xf\n"
      "v_smfmac_f32_16x16x64_bf16 v[24:27], v[16:19], a[24:31], v1 abid:1\n"
      "v_add_f32 v88, v88, v89\n"
      "v_mov_b64_dpp v[16:17], v[84:85] row_newbcast:12 row_mask:0xf bank_mask:0xf\n"
      "v_smfmac_f32_16x16x64_bf16 v[20:23], v[12:15], a[32:39], v1 abid:1\n"
      "v_add_f32 v90, v90, v91\n"
      "v_mov_b64_dpp v[18:19], v[84:85] row_newbcast:14 row_mask:0xf bank_mask:0xf\n"
      "v_smfmac_f32_16x16x64_bf16 v[24:27], v[12:15], a[40:47], v1 abid:1\n"
      "v_add_f32 v93, v88, v90\n"
      "v_mov_b64_dpp v[12:13], v[84:85] row_newbcast:1 row_mask:0xf bank_mask:0xf\n"
      "v_smfmac_f32_16x16x64_bf16 v[20:23], v[16:19], a[48:55], v1 abid:1\n"
      "v_add_f32_dpp v93, v93, v93 row_ror:8 row_mask:0xf bank_mask:0xf\n"
      "v_mov_b64_dpp v[14:15], v[84:85] row_newbcast:3 row_mask:0xf bank_mask:0xf\n"
      "v_smfmac_f32_16x16x64_bf16 v[24:27], v[16:19], a[56:63], v1 abid:1\n"
      "v_add_f32_dpp v93, v93, v93 row_ror:4 row_mask:0xf bank_mask:0xf\n"
      "v_mov_b64_dpp v[16:17], v[84:85] row_newbcast:5 row_mask:0xf bank_mask:0xf\n"
      "v_smfmac_f32_16x16x64_bf16 v[20:23], v[12:15], a[64:71], v1 abid:1\n"
      "v_add_f32_dpp v93, v93, v93 row_ror:2 row_mask:0xf bank_mask:0xf\n"
      "v_mov_b64_dpp v[18:19], v[84:85] row_newbcast:7 row_mask:0xf bank_mask:0xf\n"
      "v_smfmac_f32_16x16x64_bf16 v[24:27], v[12:15], a[72:79], v1 abid:1\n"
      "v_add_f32_dpp v93, v93, v93 row_ror:1 row_mask:0xf bank_mask:0xf\n"
      "v_mov_b64_dpp v[12:13], v[84:85] row_newbcast:9 row_mask:0xf bank_mask:0xf\n"
      "v_smfmac_f32_16x16x64_bf16 v[20:23], v[16:19], a[80:87], v1 abid:1\n"
      "v_add_f32_dpp v93, v93, v93 row_bcast:15 row_mask:0xa bank_mask:0xf\n"
      "v_mov_b64_dpp v[14:15], v[84:85] row_newbcast:11 row_mask:0xf bank_mask:0xf\n"
      "v_smfmac_f32_16x16x64_bf16 v[24:27], v[16:19], a[88:95], v1 abid:1\n"
      "v_add_f32_dpp v93, v93, v93 row_bcast:31 row_mask:0xc bank_mask:0xf\n"
      "v_mov_b64_dpp v[16:17], v[84:85] row_newbcast:13 row_mask:0xf bank_mask:0xf\n"
      "v_smfmac_f32_16x16x64_bf16 v[20:23], v[12:15], a[96:103], v1 abid:1\n"
      "ds_write2_b32 v28, v92, v93 offset0:0 offset1:4\n"
      "v_mov_b64_dpp v[18:19], v[84:85] row_newbcast:15 row_mask:0xf bank_mask:0xf\n"
      "v_smfmac_f32_16x16x64_bf16 v[24:27], v[12:15], a[104:111], v1 abid:1\n"
      "v_mov_b64_dpp v[12:13], v[86:87] row_newbcast:0 row_mask:0xf bank_mask:0xf\n"
      "v_smfmac_f32_16x16x64_bf16 v[20:23], v[16:19], a[112:119], v1 abid:1\n"
      "v_mov_b64_dpp v[14:15], v[86:87] row_newbcast:2 row_mask:0xf bank_mask:0xf\n"
      "v_smfmac_f32_16x16x64_bf16 v[24:27], v[16:19], a[120:127], v1 abid:1\n"
      "v_mov_b64_dpp v[16:17], v[86:87] row_newbcast:4 row_mask:0xf bank_mask:0xf\n"
      "v_smfmac_f32_16x16x64_bf16 v[20:23], v[12:15], a[128:135], v1 abid:1\n"
      "v_mov_b64_dpp v[18:19], v[86:87] row_newbcast:6 row_mask:0xf bank_mask:0xf\n"
      "v_smfmac_f32_16x16x64_bf16 v[24:27], v[12:15], a[136:143], v1 abid:1\n"
      "s_waitcnt lgkmcnt(0)\n"
      "s_barrier\n"
      "v_mov_b64_dpp v[12:13], v[86:87] row_newbcast:8 row_mask:0xf bank_mask:0xf\n"
      "v_smfmac_f32_16x16x64_bf16 v[20:23], v[16:19], a[144:151], v1 abid:1\n"
      "ds_read_b128 v[40:43], v29 offset:16\n"
      "v_mov_b64_dpp v[14:15], v[86:87] row_newbcast:10 row_mask:0xf bank_mask:0xf\n"
      "v_smfmac_f32_16x16x64_bf16 v[24:27], v[16:19], a[152:159], v1 abid:1\n"
      "ds_read_b128 v[28:31], v29\n"
      "v_mov_b64_dpp v[16:17], v[86:87] row_newbcast:12 row_mask:0xf bank_mask:0xf\n"
      "v_smfmac_f32_16x16x64_bf16 v[20:23], v[12:15], a[160:167], v1 abid:1\n"
      "v_mov_b64_dpp v[18:19], v[86:87] row_newbcast:14 row_mask:0xf bank_mask:0xf\n"
      "v_smfmac_f32_16x16x64_bf16 v[24:27], v[12:15], a[168:175], v1 abid:1\n"
      "v_mov_b64_dpp v[12:13], v[86:87] row_newbcast:1 row_mask:0xf bank_mask:0xf\n"
      "v_smfmac_f32_16x16x64_bf16 v[20:23], v[16:19], a[176:183], v1 abid:1\n"
      "v_mov_b64_dpp v[14:15], v[86:87] row_newbcast:3 row_mask:0xf bank_mask:0xf\n"
      "v_smfmac_f32_16x16x64_bf16 v[24:27], v[16:19], a[184:191], v1 abid:1\n"
      "v_mov_b64_dpp v[16:17], v[86:87] row_newbcast:5 row_mask:0xf bank_mask:0xf\n"
      "v_smfmac_f32_16x16x64_bf16 v[20:23], v[12:15], a[192:199], v1 abid:1\n"
      "v_mov_b64_dpp v[18:19], v[86:87] row_newbcast:7 row_mask:0xf bank_mask:0xf\n"
      "v_smfmac_f32_16x16x64_bf16 v[24:27], v[12:15], a[200:207], v1 abid:1\n"
      "v_mov_b64_dpp v[12:13], v[86:87] row_newbcast:9 row_mask:0xf bank_mask:0xf\n"
      "v_smfmac_f32_16x16x64_bf16 v[20:23], v[16:19], a[208:215], v1 abid:1\n"
      "v_mov_b64_dpp v[14:15], v[86:87] row_newbcast:11 row_mask:0xf bank_mask:0xf\n"
      "v_smfmac_f32_16x16x64_bf16 v[24:27], v[16:19], a[216:223], v1 abid:1\n"
      "v_mov_b64_dpp v[16:17], v[86:87] row_newbcast:13 row_mask:0xf bank_mask:0xf\n"
      "v_smfmac_f32_16x16x64_bf16 v[20:23], v[12:15], a[224:231], v1 abid:1\n"
      "v_mov_b64_dpp v[18:19], v[86:87] row_newbcast:15 row_mask:0xf bank_mask:0xf\n"
      "v_smfmac_f32_16x16x64_bf16 v[24:27], v[12:15], a[232:239], v1 abid:1\n"
      "s_nop 0\n"
      "v_smfmac_f32_16x16x64_bf16 v[20:23], v[16:19], a[240:247], v1 abid:1\n"
      "v_smfmac_f32_16x16x64_bf16 v[24:27], v[16:19], a[248:255], v1 abid:1\n"
      ".Lk_tail:\n"
      "s_waitcnt lgkmcnt(0)\n"
      "v_max3_f32 v92, v28, v29, v30\n"
      "v_max_f32 v92, v92, v31\n"
      "v_sub_f32 v28, v28, v92\n"
      "v_sub_f32 v29, v29, v92\n"
      "v_sub_f32 v30, v30, v92\n"
      "v_sub_f32 v31, v31, v92\n"
      "v_cndmask_b32 v20, v20, v22, s[72:73]\n"
      "v_cndmask_b32 v21, v21, v23, s[72:73]\n"
      "v_cndmask_b32 v24, v24, v26, s[72:73]\n"
      "v_cndmask_b32 v25, v25, v27, s[72:73]\n"
      "v_cndmask_b32 v20, v20, v24, s[8:9]\n"
      "v_cndmask_b32 v21, v21, v25, s[8:9]\n"
      "ds_write_b64 v99, v[20:21]\n"
      "v_exp_f32 v28, v28\n"
      "v_exp_f32 v29, v29\n"
      "v_exp_f32 v30, v30\n"
      "v_exp_f32 v31, v31\n"
      "s_cmp_lg_u32 s56, 0\n"
      "s_waitcnt lgkmcnt(0)\n"
      "s_barrier\n"
      "s_cbranch_scc1 .Lk_tail_ml\n"
      "ds_read_b64 v[32:33], v100 offset:0\n"
      "ds_read_b64 v[34:35], v100 offset:512\n"
      "ds_read_b64 v[36:37], v100 offset:1024\n"
      "ds_read_b64 v[38:39], v100 offset:1536\n"
      "s_waitcnt lgkmcnt(3)\n"
      "v_pk_mul_f32 v[32:33], v[32:33], v[28:29] op_sel:[0,0] op_sel_hi:[1,0]\n"
      "s_waitcnt lgkmcnt(2)\n"
      "v_pk_fma_f32 v[32:33], v[34:35], v[28:29], v[32:33] op_sel:[0,1,0] op_sel_hi:[1,1,1]\n"
      "s_waitcnt lgkmcnt(1)\n"
      "v_pk_fma_f32 v[32:33], v[36:37], v[30:31], v[32:33] op_sel:[0,0,0] op_sel_hi:[1,0,1]\n"
      "s_waitcnt lgkmcnt(0)\n"
      "v_pk_fma_f32 v[32:33], v[38:39], v[30:31], v[32:33] op_sel:[0,1,0] op_sel_hi:[1,1,1]\n"
      "v_cvt_pk_bf16_f32 v32, v32, v33\n"
      "buffer_store_dword v32, v98, s[40:43], s12 offen sc1\n"
      ".Lk_roles:\n"
      "s_cmp_lg_u32 s61, 0\n"
      "s_cbranch_scc1 .Lk_other\n"
      "s_branch .Lk_nb_112\n"
      ".Lk_na_111:\n"
      "s_sleep 4\n"
      ".Lk_nb_112:\n"
      "s_memrealtime s[6:7]\n"
      "s_waitcnt lgkmcnt(0)\n"
      "s_sub_u32 s23, s6, s19\n"
      "s_cmp_lt_i32 s23, -24\n"
      "s_cbranch_scc1 .Lk_na_111\n"
      ".Lk_un_107:\n"
      "s_memrealtime s[84:85]\n"
      "s_waitcnt lgkmcnt(2)\n"
      "s_sub_u32 s23, s6, s19\n"
      "s_cmp_lt_i32 s23, -3\n"
      "s_cbranch_scc0 .Lk_uo_108\n"
      "s_nop 4\n"
      "s_memrealtime s[6:7]\n"
      "s_waitcnt lgkmcnt(2)\n"
      "s_sub_u32 s23, s84, s19\n"
      "s_cmp_lt_i32 s23, -3\n"
      "s_cbranch_scc1 .Lk_un_107\n"
      "s_nop 4\n"
      ".Lk_uo_108:\n"
      ".Lk_uf_109:\n"
      "s_addk_i32 s23, 1\n"
      "s_cmp_lt_i32 s23, 1\n"
      "s_cbranch_scc1 .Lk_uf_109\n"
      "buffer_load_dwordx4 v[60:63], v3, s[40:43], s74 offen sc1\n"
      "buffer_load_dwordx4 v[12:15], v8, s[40:43], s12 offen sc1\n"
      "buffer_load_dwordx4 v[16:19], v8, s[40:43], s12 offen offset:256 sc1\n"
      "v_xor_b32 v70, 32, v9\n"
      "s_and_b32 s68, s57, 0x1\n"
      "s_lshl_b32 s68, s68, 8\n"
      "s_and_b32 s79, s57, 0x2\n"
      "s_lshl_b32 s79, s79, 1\n"
      "s_add_u32 s68, s68, s79\n"
      "s_add_u32 s68, s68, 0x26800\n"
      "v_bfe_u32 v11, v0, 5, 1\n"
      "v_lshl_add_u32 v64, v11, 9, s68\n"
      "v_bfe_u32 v11, v0, 0, 5\n"
      "v_lshl_add_u32 v64, v11, 3, v64\n"
      "s_mov_b32 s68, 157696\n"
      "v_lshl_add_u32 v65, v0, 4, s68\n"
      "s_and_b32 s68, s57, 0x80\n"
      "s_lshl_b32 s68, s68, 7\n"
      "s_and_b32 s79, s57, 0x60\n"
      "s_lshl_b32 s79, s79, 6\n"
      "s_add_u32 s68, s68, s79\n"
      "s_and_b32 s79, s57, 0x1c\n"
      "s_lshl_b32 s79, s79, 6\n"
      "s_add_u32 s68, s68, s79\n"
      "s_and_b32 s79, s57, 0x100\n"
      "s_lshr_b32 s79, s79, 1\n"
      "s_add_u32 s68, s68, s79\n"
      "s_and_b32 s79, s57, 0x1\n"
      "s_lshl_b32 s79, s79, 13\n"
      "s_add_u32 s68, s68, s79\n"
      "s_add_u32 s68, s68, 0x88000\n"
      "s_xor_b32 s68, s68, 2048\n"
      "v_bfe_u32 v11, v0, 5, 1\n"
      "v_lshl_add_u32 v66, v11, 15, s68\n"
      "v_bfe_u32 v11, v0, 0, 5\n"
      "v_lshl_add_u32 v66, v11, 2, v66\n"
      "s_cmp_lg_u32 s62, 0\n"
      "s_cbranch_scc1 .Lk_s2w_113\n"
      "s_waitcnt vmcnt(3)\n"
      "s_barrier\n"
      ".Lk_s2w_113:\n"
      "ds_read_b64_tr_b16 v[28:29], v9 offset:0\n"
      "ds_read_b64_tr_b16 v[30:31], v9 offset:1024\n"
      "ds_read_b64_tr_b16 v[32:33], v9 offset:4096\n"
      "ds_read_b64_tr_b16 v[34:35], v9 offset:5120\n"
      "ds_read_b64_tr_b16 v[44:45], v70 offset:0\n"
      "ds_read_b64_tr_b16 v[46:47], v70 offset:1024\n"
      "ds_read_b64_tr_b16 v[48:49], v70 offset:4096\n"
      "ds_read_b64_tr_b16 v[50:51], v70 offset:5120\n"
      "ds_read_b64_tr_b16 v[36:37], v9 offset:8192\n"
      "ds_read_b64_tr_b16 v[38:39], v9 offset:9216\n"
      "ds_read_b64_tr_b16 v[40:41], v9 offset:12288\n"
      "ds_read_b64_tr_b16 v[42:43], v9 offset:13312\n"
      "s_waitcnt lgkmcnt(8)\n"
      "ds_read_b64_tr_b16 v[52:53], v70 offset:8192\n"
      "ds_read_b64_tr_b16 v[54:55], v70 offset:9216\n"
      "ds_read_b64_tr_b16 v[56:57], v70 offset:12288\n"
      "ds_read_b64_tr_b16 v[58:59], v70 offset:13312\n"
      ".Lk_o_wait:\n"
      "v_mov_b64 v[20:21], 0\n"
      "v_mov_b64 v[22:23], 0\n"
      "v_mov_b64 v[24:25], 0\n"
      "v_mov_b64 v[26:27], 0\n"
      "s_waitcnt vmcnt(2)\n"
      "v_max_f32_dpp v71, v60, v60 row_ror:4 row_mask:0xf bank_mask:0xf\n"
      "v_cmp_ne_u32 vcc, s70, v61\n"
      "v_cmp_ne_u32 s[68:69], s70, v63\n"
      "v_max_f32_dpp v71, v71, v71 row_ror:8 row_mask:0xf bank_mask:0xf\n"
      "v_sub_f32 v72, v60, v71\n"
      "s_or_b64 vcc, vcc, s[68:69]\n"
      "v_exp_f32 v72, v72\n"
      "v_cndmask_b32 v11, 0, -1, vcc\n"
      "s_waitcnt lgkmcnt(0)\n"
      "v_mul_f32 v73, v72, v62\n"
      "s_waitcnt vmcnt(1)\n"
      "v_smfmac_f32_16x16x64_bf16 v[20:23], v[12:15], v[28:35], v1\n"
      "v_add_f32_dpp v73, v73, v73 row_ror:4 row_mask:0xf bank_mask:0xf\n"
      "v_smfmac_f32_16x16x64_bf16 v[24:27], v[12:15], v[44:51], v1\n"
      "v_max3_u32 v11, v11, v12, v13\n"
      "v_add_f32_dpp v73, v73, v73 row_ror:8 row_mask:0xf bank_mask:0xf\n"
      "v_rcp_f32 v73, v73\n"
      "v_max3_u32 v11, v11, v14, v15\n"
      "v_mul_f32 v72, v72, v73\n"
      "s_waitcnt vmcnt(0)\n"
      "v_smfmac_f32_16x16x64_bf16 v[20:23], v[16:19], v[36:43], v1\n"
      "v_mov_b32_dpp v67, v72 row_newbcast:0 row_mask:0x5 bank_mask:0xf\n"
      "v_smfmac_f32_16x16x64_bf16 v[24:27], v[16:19], v[52:59], v1\n"
      "v_mov_b32_dpp v68, v72 row_newbcast:4 row_mask:0x5 bank_mask:0xf\n"
      "v_max3_u32 v11, v11, v16, v17\n"
      "v_max3_u32 v11, v11, v18, v19\n"
      "v_mov_b32_dpp v67, v72 row_newbcast:8 row_mask:0xa bank_mask:0xf\n"
      "v_mov_b32_dpp v68, v72 row_newbcast:12 row_mask:0xa bank_mask:0xf\n"
      "v_cmp_lt_u32 vcc, s67, v11\n"
      "s_cbranch_vccnz .Lk_o_retry\n"
      "v_add_f32 v20, v20, v21\n"
      "v_add_f32 v22, v22, v23\n"
      "v_add_f32 v24, v24, v25\n"
      "v_add_f32 v26, v26, v27\n"
      "v_mul_f32 v20, v67, v20\n"
      "v_mul_f32 v24, v67, v24\n"
      "v_fmac_f32 v20, v68, v22\n"
      "v_fmac_f32 v24, v68, v26\n"
      "s_cmp_ge_u32 s56, 2\n"
      "s_nop 0\n"
      "v_permlane16_swap_b32 v20, v24\n"
      "v_add_f32 v20, v20, v24\n"
      "ds_write_b32 v64, v20\n"
      "s_waitcnt lgkmcnt(0)\n"
      "s_barrier\n"
      "s_cbranch_scc1 .Lk_s2_done\n"
      "ds_read_b128 v[20:23], v65\n"
      "s_waitcnt lgkmcnt(0)\n"
      "v_add_f32 v20, v20, v21\n"
      "v_add_f32 v22, v22, v23\n"
      "v_cvt_pk_bf16_f32 v20, v20, v22\n"
      "buffer_store_dword v20, v66, s[40:43], s62 offen sc1\n"
      ".Lk_s2_done:\n"
      "s_branch .Lk_next\n"
      ".Lk_o_retry:\n"
      "buffer_load_dwordx4 v[60:63], v3, s[40:43], s74 offen sc1\n"
      "buffer_load_dwordx4 v[12:15], v8, s[40:43], s12 offen sc1\n"
      "buffer_load_dwordx4 v[16:19], v8, s[40:43], s12 offen offset:256 sc1\n"
      "s_sub_u32 s66, s66, 1\n"
      "s_cselect_b32 s67, -1, s67\n"
      "s_nop 4\n"
      "s_branch .Lk_o_wait\n"
      ".Lk_tail_ml:\n"
      "s_cmp_lg_u32 s56, 2\n"
      "s_cbranch_scc1 .Lk_tail_rest\n"
      "v_mul_f32 v94, v28, v40\n"
      "v_mov_b32 v93, s70\n"
      "v_fmac_f32 v94, v29, v41\n"
      "v_fmac_f32 v94, v30, v42\n"
      "v_fmac_f32 v94, v31, v43\n"
      "v_mov_b32 v95, s70\n"
      "buffer_store_dwordx4 v[92:95], v98, s[40:43], s74 offen sc1\n"
      ".Lk_tail_rest:\n"
      "s_branch .Lk_roles\n"
      ".Lk_other:\n"
      "s_waitcnt lgkmcnt(0)\n"
      "s_cmp_eq_u32 s61, 2\n"
      "s_cbranch_scc1 .Lk_red\n"
      "s_cmp_eq_u32 s61, 3\n"
      "s_cbranch_scc1 .Lk_idle\n"
      "s_lshl_b32 s17, s70, 1\n"
      "s_cmp_lg_u32 s62, 0\n"
      "s_addc_u32 s17, s17, 0\n"
      "s_cmp_lg_u32 s62, 0\n"
      "s_cbranch_scc1 .Lk_fl_114\n"
      "s_barrier\n"
      ".Lk_fl_114:\n"
      "s_and_b32 s68, s57, 0x1\n"
      "s_lshl_b32 s68, s68, 10\n"
      "s_and_b32 s79, s57, 0x60\n"
      "s_lshl_b32 s79, s79, 3\n"
      "s_add_u32 s68, s68, s79\n"
      "v_bfe_u32 v11, v0, 0, 4\n"
      "v_lshl_add_u32 v89, v11, 4, s68\n"
      "v_bfe_u32 v11, v0, 4, 1\n"
      "v_lshl_add_u32 v89, v11, 3, v89\n"
      "buffer_load_dwordx2 v[28:29], v89, s[44:47], 0 offen sc1\n"
      "s_and_b32 s68, s57, 0x1\n"
      "s_lshl_b32 s68, s68, 15\n"
      "s_and_b32 s79, s57, 0x40\n"
      "s_lshl_b32 s79, s79, 8\n"
      "s_add_u32 s68, s68, s79\n"
      "s_and_b32 s79, s57, 0x20\n"
      "s_lshl_b32 s79, s79, 7\n"
      "s_add_u32 s68, s68, s79\n"
      "s_add_u32 s68, s68, 0x88000\n"
      "v_bfe_u32 v11, v0, 3, 1\n"
      "v_lshl_add_u32 v88, v11, 11, s68\n"
      "v_bfe_u32 v11, v0, 4, 2\n"
      "v_lshl_add_u32 v88, v11, 7, v88\n"
      "v_bfe_u32 v11, v0, 0, 3\n"
      "v_lshl_add_u32 v88, v11, 4, v88\n"
      "s_and_b32 s68, s57, 0x1\n"
      "s_lshl_b32 s68, s68, 8\n"
      "s_add_u32 s68, s68, 0x26c00\n"
      "v_bfe_u32 v11, v0, 0, 4\n"
      "v_lshl_add_u32 v90, v11, 4, s68\n"
      "v_bfe_u32 v11, v0, 4, 1\n"
      "v_lshl_add_u32 v90, v11, 3, v90\n"
      "s_mov_b32 s68, 158720\n"
      "v_bfe_u32 v11, v0, 2, 1\n"
      "v_lshl_add_u32 v91, v11, 8, s68\n"
      "v_bfe_u32 v11, v0, 4, 2\n"
      "v_lshl_add_u32 v91, v11, 6, v91\n"
      "v_bfe_u32 v11, v0, 0, 2\n"
      "v_lshl_add_u32 v91, v11, 4, v91\n"
      "s_and_b32 s68, s57, 0x3\n"
      "s_lshl_b32 s68, s68, 3\n"
      "s_add_u32 s68, s68, 0x27800\n"
      "v_bfe_u32 v11, v0, 4, 1\n"
      "v_lshl_add_u32 v92, v11, 2, s68\n"
      "s_mov_b32 s68, 161792\n"
      "v_bfe_u32 v11, v0, 0, 2\n"
      "v_lshl_add_u32 v93, v11, 2, s68\n"
      "s_and_b32 s69, s75, 0x7c\n"
      "s_lshl_b32 s69, s69, 12\n"
      "s_add_u32 s69, s69, 0x200000\n"
      "s_xor_b32 s68, s74, 8192\n"
      "s_add_u32 s69, s69, s68\n"
      "s_and_b32 s68, s57, 0x3\n"
      "s_lshl_b32 s68, s68, 11\n"
      "s_and_b32 s79, s57, 0x60\n"
      "s_lshl_b32 s79, s79, 4\n"
      "s_add_u32 s68, s68, s79\n"
      "s_add_u32 s68, s68, s69\n"
      "v_bfe_u32 v11, v0, 0, 5\n"
      "v_lshl_add_u32 v94, v11, 4, s68\n"
      "v_bfe_u32 v11, v0, 5, 1\n"
      "v_lshl_add_u32 v94, v11, 30, v94\n"
      "s_and_b32 s68, s57, 0x60\n"
      "s_lshl_b32 s68, s68, 6\n"
      "s_add_u32 s68, s68, s69\n"
      "v_bfe_u32 v11, v0, 3, 1\n"
      "v_lshl_add_u32 v95, v11, 10, s68\n"
      "v_bfe_u32 v11, v0, 2, 1\n"
      "v_lshl_add_u32 v95, v11, 8, v95\n"
      "v_bfe_u32 v11, v0, 4, 2\n"
      "v_lshl_add_u32 v95, v11, 6, v95\n"
      "v_bfe_u32 v11, v0, 0, 2\n"
      "v_lshl_add_u32 v95, v11, 4, v95\n"
      "s_and_b32 s68, s75, 0x7c\n"
      "s_lshl_b32 s68, s68, 12\n"
      "s_and_b32 s79, s75, 0x3\n"
      "s_lshl_b32 s79, s79, 11\n"
      "s_add_u32 s68, s68, s79\n"
      "s_add_u32 s68, s68, 0x10000\n"
      "v_bfe_u32 v11, v0, 5, 1\n"
      "v_lshl_add_u32 v96, v11, 30, s68\n"
      "v_bfe_u32 v11, v0, 0, 3\n"
      "v_lshl_add_u32 v96, v11, 4, v96\n"
      "s_lshl1_add_u32 s69, s56, s75\n"
      "v_bfe_u32 v11, v0, 4, 1\n"
      "v_lshl_add_u32 v87, v11, 3, s69\n"
      "v_bfe_u32 v11, v0, 3, 1\n"
      "v_lshl_add_u32 v87, v11, 0, v87\n"
      "v_and_b32 v87, 15, v87\n"
      "v_lshl_add_u32 v96, v87, 7, v96\n"
      "v_bfe_u32 v11, v0, 4, 1\n"
      "v_lshl_add_u32 v87, v11, 2, s56\n"
      "v_bfe_u32 v87, s21, v87, 1\n"
      "v_lshl_add_u32 v96, v87, 13, v96\n"
      "s_mov_b32 s68, 1523712\n"
      "v_bfe_u32 v11, v0, 1, 5\n"
      "v_lshl_add_u32 v101, v11, 5, s68\n"
      "s_and_b32 s68, s75, 0x40\n"
      "s_lshl_b32 s68, s68, 8\n"
      "s_and_b32 s79, s75, 0x3f\n"
      "s_lshl_b32 s79, s79, 7\n"
      "s_add_u32 s68, s68, s79\n"
      "s_add_u32 s68, s68, 0x1d0000\n"
      "s_or_b32 s76, s56, s62\n"
      "v_bfe_u32 v11, v0, 0, 4\n"
      "v_lshl_add_u32 v31, v11, 3, s68\n"
      "v_bfe_u32 v11, v0, 4, 1\n"
      "v_lshl_add_u32 v31, v11, 13, v31\n"
      "v_bfe_u32 v11, v0, 5, 1\n"
      "v_lshl_add_u32 v31, v11, 30, v31\n"
      "v_mov_b32 v11, 0x3f80\n"
      "v_mov_b64 v[20:21], 0\n"
      "v_mov_b64 v[22:23], 0\n"
      "s_nop 0\n"
      "v_mov_b32_dpp v20, v11 quad_perm:[0,1,2,3] row_mask:0xf bank_mask:0x5\n"
      "v_mov_b32_dpp v21, v11 quad_perm:[0,1,2,3] row_mask:0xf bank_mask:0xa\n"
      "v_mov_b32_dpp v22, v11 quad_perm:[0,1,2,3] row_mask:0xf bank_mask:0x5\n"
      "v_mov_b32_dpp v23, v11 quad_perm:[0,1,2,3] row_mask:0xf bank_mask:0xa\n"
      "v_mov_b32 v97, 0xbb000000\n"
      "v_mov_b32 v11, 0x3b000000\n"
      "v_cndmask_b32 v97, v97, v11, s[72:73]\n"
      "s_mov_b32 s68, 122880\n"
      "s_and_b32 s69, s57, 0x3\n"
      "s_lshl_b32 s69, s69, 5\n"
      "v_bfe_u32 v11, v0, 5, 1\n"
      "v_lshl_add_u32 v8, v11, 12, s68\n"
      "v_bfe_u32 v11, v0, 3, 1\n"
      "v_lshl_add_u32 v8, v11, 9, v8\n"
      "v_bfe_u32 v11, v0, 3, 1\n"
      "v_lshl_add_u32 v8, v11, 6, v8\n"
      "v_bfe_u32 v11, v0, 4, 1\n"
      "v_lshl_add_u32 v8, v11, 8, v8\n"
      "v_bfe_u32 v11, v0, 4, 1\n"
      "v_lshl_add_u32 v8, v11, 5, v8\n"
      "v_bfe_u32 v11, v0, 2, 1\n"
      "v_lshl_add_u32 v8, v11, 7, v8\n"
      "v_bfe_u32 v11, v0, 0, 2\n"
      "v_lshl_add_u32 v8, v11, 3, v8\n"
      "v_xor_b32 v8, s69, v8\n"
      "s_add_u32 s68, s58, 16384\n"
      "v_bfe_u32 v11, v0, 4, 2\n"
      "v_lshl_add_u32 v10, v11, 9, s68\n"
      "v_bfe_u32 v11, v0, 0, 4\n"
      "v_lshl_add_u32 v10, v11, 4, v10\n"
      "v_mov_b32 v40, 0x3f803f80\n"
      "v_mov_b32 v41, 0x3f803f80\n"
      "v_mov_b32 v42, 0x3f803f80\n"
      "v_mov_b32 v43, 0x3f803f80\n"
      "v_mov_b64 v[32:33], 0\n"
      "v_mov_b64 v[34:35], 0\n"
      "v_mov_b64 v[36:37], 0\n"
      "v_mov_b64 v[38:39], 0\n"
      "v_mov_b64 v[76:77], 0\n"
      "v_mov_b64 v[78:79], 0\n"
      "v_mov_b64 v[80:81], 0\n"
      "v_mov_b64 v[82:83], 0\n"
      "s_cmp_ge_u32 s56, 2\n"
      "s_cbranch_scc1 .Lk_fv_116\n"
      "s_add_u32 s19, s18, 172\n"
      "s_add_u32 s11, s18, 569\n"
      "s_cmp_eq_u32 s62, 0\n"
      "s_cselect_b32 s19, s19, s11\n"
      "s_branch .Lk_nb_122\n"
      ".Lk_na_121:\n"
      "s_sleep 4\n"
      ".Lk_nb_122:\n"
      "s_memrealtime s[6:7]\n"
      "s_waitcnt lgkmcnt(0)\n"
      "s_sub_u32 s23, s6, s19\n"
      "s_cmp_lt_i32 s23, -24\n"
      "s_cbranch_scc1 .Lk_na_121\n"
      ".Lk_un_117:\n"
      "s_memrealtime s[84:85]\n"
      "s_waitcnt lgkmcnt(2)\n"
      "s_sub_u32 s23, s6, s19\n"
      "s_cmp_lt_i32 s23, -3\n"
      "s_cbranch_scc0 .Lk_uo_118\n"
      "s_nop 4\n"
      "s_memrealtime s[6:7]\n"
      "s_waitcnt lgkmcnt(2)\n"
      "s_sub_u32 s23, s84, s19\n"
      "s_cmp_lt_i32 s23, -3\n"
      "s_cbranch_scc1 .Lk_un_117\n"
      "s_nop 4\n"
      ".Lk_uo_118:\n"
      ".Lk_uf_119:\n"
      "s_addk_i32 s23, 1\n"
      "s_cmp_lt_i32 s23, 1\n"
      "s_cbranch_scc1 .Lk_uf_119\n"
      "buffer_load_dwordx4 v[60:63], v88, s[40:43], s74 offen sc1\n"
      "buffer_load_dwordx4 v[64:67], v88, s[40:43], s74 offen offset:512 sc1\n"
      "buffer_load_dwordx4 v[68:71], v88, s[40:43], s74 offen offset:1024 sc1\n"
      "buffer_load_dwordx4 v[72:75], v88, s[40:43], s74 offen offset:1536 sc1\n"
      "s_waitcnt vmcnt(4)\n"
      "s_branch .Lk_fy_123\n"
      ".Lk_fv_116:\n"
      "s_waitcnt vmcnt(0)\n"
      ".Lk_fy_123:\n"
      "s_cmp_lg_u32 s62, 0\n"
      "s_cbranch_scc1 .Lk_fu_115\n"
      "s_barrier\n"
      ".Lk_fu_115:\n"
      "ds_read_b64_tr_b16 v[44:45], v8 offset:0\n"
      "ds_read_b64_tr_b16 v[46:47], v8 offset:1024\n"
      "ds_read_b64_tr_b16 v[48:49], v8 offset:8192\n"
      "ds_read_b64_tr_b16 v[50:51], v8 offset:9216\n"
      "ds_read_b64_tr_b16 v[52:53], v8 offset:2048\n"
      "ds_read_b64_tr_b16 v[54:55], v8 offset:3072\n"
      "ds_read_b64_tr_b16 v[56:57], v8 offset:10240\n"
      "ds_read_b64_tr_b16 v[58:59], v8 offset:11264\n"
      "s_waitcnt lgkmcnt(0)\n"
      "v_smfmac_f32_16x16x64_bf16 v[32:35], v[40:43], v[44:51], v1\n"
      "v_smfmac_f32_16x16x64_bf16 v[36:39], v[40:43], v[52:59], v1\n"
      "s_add_u32 s19, s18, 230\n"
      "s_add_u32 s11, s18, 625\n"
      "s_cmp_eq_u32 s62, 0\n"
      "s_cselect_b32 s19, s19, s11\n"
      "v_mov_b32 v41, s17\n"
      "v_mov_b32 v43, s17\n"
      "s_nop 0\n"
      "v_add_f32 v32, v32, v33\n"
      "v_add_f32 v38, v38, v39\n"
      "v_add_f32 v84, v32, v38\n"
      "v_mov_b64 v[32:33], 0\n"
      "v_mov_b64 v[34:35], 0\n"
      "v_mov_b64 v[36:37], 0\n"
      "v_mov_b64 v[38:39], 0\n"
      "s_cmp_ge_u32 s56, 2\n"
      "s_cbranch_scc1 .Lk_x_join\n"
      ".Lk_p_wait:\n"
      "v_lshlrev_b32 v24, 16, v28\n"
      "v_and_b32 v25, 0xffff0000, v28\n"
      "v_lshlrev_b32 v26, 16, v29\n"
      "v_and_b32 v27, 0xffff0000, v29\n"
      "v_max_u32 v11, v28, v29\n"
      "s_waitcnt vmcnt(3)\n"
      "v_max3_u32 v11, v11, v60, v61\n"
      "v_max3_u32 v11, v11, v62, v63\n"
      "s_waitcnt vmcnt(2)\n"
      "v_smfmac_f32_16x16x64_bf16 v[24:27], v[20:23], v[60:67], v1 abid:1\n"
      "v_max3_u32 v11, v11, v64, v65\n"
      "v_max3_u32 v11, v11, v66, v67\n"
      "s_waitcnt vmcnt(1)\n"
      "v_max3_u32 v11, v11, v68, v69\n"
      "v_max3_u32 v11, v11, v70, v71\n"
      "s_waitcnt vmcnt(0)\n"
      "v_smfmac_f32_16x16x64_bf16 v[24:27], v[20:23], v[68:75], v1 abid:1\n"
      "v_max3_u32 v11, v11, v72, v73\n"
      "v_max3_u32 v11, v11, v74, v75\n"
      "v_cmp_lt_u32 vcc, s67, v11\n"
      "s_cbranch_vccnz .Lk_p_retry\n"
      "s_nop 3\n"
      "v_cvt_pk_bf16_f32 v28, v24, v25\n"
      "v_cvt_pk_bf16_f32 v29, v26, v27\n"
      "ds_write_b64 v90, v[28:29]\n"
      "v_add_f32 v85, v24, v25\n"
      "v_mul_f32 v86, v24, v24\n"
      "v_add_f32 v85, v85, v26\n"
      "v_fmac_f32 v86, v25, v25\n"
      "v_fmac_f32 v86, v26, v26\n"
      "v_fmac_f32 v86, v27, v27\n"
      "v_add_f32 v85, v85, v27\n"
      "s_nop 1\n"
      "v_permlane16_swap_b32 v85, v86\n"
      "v_add_f32 v85, v85, v86\n"
      "v_mul_f32 v85, v85, v97\n"
      "s_nop 1\n"
      "v_add_f32_dpp v85, v85, v85 row_ror:8 row_mask:0xf bank_mask:0xf\n"
      "s_waitcnt lgkmcnt(0)\n"
      ".Lk_x_join:\n"
      "s_barrier\n"
      "ds_read_b128 v[12:15], v91\n"
      "s_memrealtime s[6:7]\n"
      "v_add_f32_dpp v85, v85, v85 row_ror:4 row_mask:0xf bank_mask:0xf\n"
      "s_nop 1\n"
      "v_add_f32_dpp v85, v85, v85 row_ror:2 row_mask:0xf bank_mask:0xf\n"
      "s_nop 1\n"
      "v_add_f32_dpp v85, v85, v85 row_ror:1 row_mask:0xf bank_mask:0xf\n"
      "ds_write_b32 v92, v85\n"
      "s_waitcnt lgkmcnt(1)\n"
      "v_smfmac_f32_16x16x64_bf16 v[32:35], v[12:15], v[44:51], v1\n"
      "v_smfmac_f32_16x16x64_bf16 v[36:39], v[12:15], v[52:59], v1\n"
      "s_cmp_eq_u32 s76, 1\n"
      "s_waitcnt lgkmcnt(0)\n"
      "s_barrier\n"
      "ds_read_b32 v86, v93\n"
      "s_cbranch_scc1 .Lk_eta_put\n"
      ".Lk_eta_done:\n"
      "s_nop 1\n"
      "v_add_f32 v32, v32, v33\n"
      "v_add_f32 v38, v38, v39\n"
      "v_add_f32 v40, v32, v38\n"
      "s_waitcnt lgkmcnt(0)\n"
      "v_cndmask_b32 v42, v84, v86, s[72:73]\n"
      "buffer_store_dwordx4 v[40:43], v94, s[40:43], 0 offen sc1\n"
      "s_branch .Lk_nb_129\n"
      ".Lk_na_128:\n"
      "s_sleep 4\n"
      ".Lk_nb_129:\n"
      "s_memrealtime s[6:7]\n"
      "s_waitcnt lgkmcnt(0)\n"
      "s_sub_u32 s23, s6, s19\n"
      "s_cmp_lt_i32 s23, -24\n"
      "s_cbranch_scc1 .Lk_na_128\n"
      ".Lk_un_124:\n"
      "s_memrealtime s[84:85]\n"
      "s_waitcnt lgkmcnt(2)\n"
      "s_sub_u32 s23, s6, s19\n"
      "s_cmp_lt_i32 s23, -3\n"
      "s_cbranch_scc0 .Lk_uo_125\n"
      "s_nop 4\n"
      "s_memrealtime s[6:7]\n"
      "s_waitcnt lgkmcnt(2)\n"
      "s_sub_u32 s23, s84, s19\n"
      "s_cmp_lt_i32 s23, -3\n"
      "s_cbranch_scc1 .Lk_un_124\n"
      "s_nop 4\n"
      ".Lk_uo_125:\n"
      ".Lk_uf_126:\n"
      "s_addk_i32 s23, 1\n"
      "s_cmp_lt_i32 s23, 1\n"
      "s_cbranch_scc1 .Lk_uf_126\n"
      "buffer_load_dwordx4 v[12:15], v95, s[40:43], 0 offen sc1\n"
      "buffer_load_dwordx4 v[16:19], v95, s[40:43], 0 offen offset:512 sc1\n"
      "ds_read_b64 v[60:61], v10 offset:0\n"
      "ds_read_b64 v[62:63], v10 offset:256\n"
      "ds_read_b64 v[64:65], v10 offset:2048\n"
      "ds_read_b64 v[66:67], v10 offset:2304\n"
      "ds_read_b64 v[68:69], v10 offset:8\n"
      "ds_read_b64 v[70:71], v10 offset:264\n"
      "ds_read_b64 v[72:73], v10 offset:2056\n"
      "ds_read_b64 v[74:75], v10 offset:2312\n"
      "s_nop 7\n"
      "buffer_load_dwordx2 v[20:21], v101, s[40:43], s63 offen sc1\n"
      ".Lk_z_wait:\n"
      "s_bfe_i32 s69, s67, 0x10000\n"
      "s_waitcnt vmcnt(2)\n"
      "v_xor_b32 v11, s17, v13\n"
      "v_bitop3_b32 v11, v15, s17, v11 bitop3:0xbe\n"
      "s_waitcnt vmcnt(1)\n"
      "v_add_f32 v25, v14, v18\n"
      "v_add_f32 v24, v12, v16\n"
      "v_bitop3_b32 v11, v17, s17, v11 bitop3:0xbe\n"
      "v_add_f32_dpp v25, v25, v25 row_ror:8 row_mask:0xf bank_mask:0xf\n"
      "v_add_f32_dpp v24, v24, v24 row_ror:8 row_mask:0xf bank_mask:0xf\n"
      "v_bitop3_b32 v11, v19, s17, v11 bitop3:0xbe\n"
      "v_mov_b32_dpp v26, v25 row_newbcast:4 row_mask:0xf bank_mask:0x5\n"
      "v_mov_b32_dpp v27, v25 row_newbcast:5 row_mask:0xf bank_mask:0x5\n"
      "v_cmp_lt_u32 vcc, s69, v11\n"
      "v_mov_b32_dpp v26, v25 row_newbcast:6 row_mask:0xf bank_mask:0xa\n"
      "v_mov_b32_dpp v27, v25 row_newbcast:7 row_mask:0xf bank_mask:0xa\n"
      "s_cbranch_vccnz .Lk_z_retry\n"
      "v_fma_f32 v27, -v26, v26, v27\n"
      "v_max_f32 v27, 0, v27\n"
      "v_add_f32 v27, 0x3727c5ac, v27\n"
      "v_rsq_f32 v27, v27\n"
      "v_mov_b32_dpp v25, v25 row_ror:4 row_mask:0xf bank_mask:0xa\n"
      "v_fmac_f32 v24, v26, v25\n"
      "v_fma_f32 v24, v24, v27, v3\n"
      "v_mul_f32 v27, 0xbdd2d3e8, v24\n"
      "v_fmaak_f32 v27, v27, v24, 0xc0135761\n"
      "v_mul_f32 v27, v27, v24\n"
      "v_exp_f32 v27, v27\n"
      "s_nop 0\n"
      "v_add_f32 v27, 1.0, v27\n"
      "v_rcp_f32 v27, v27\n"
      "s_nop 0\n"
      "v_mul_f32 v24, v24, v27\n"
      "v_cvt_pk_bf16_f32 v24, v24, 0\n"
      "s_waitcnt lgkmcnt(0)\n"
      "s_nop 0\n"
      "v_mov_b32_dpp v12, v24 quad_perm:[0,0,0,0] row_mask:0xf bank_mask:0xf\n"
      "v_mov_b32_dpp v13, v24 quad_perm:[1,1,1,1] row_mask:0xf bank_mask:0xf\n"
      "v_mov_b32_dpp v14, v24 quad_perm:[2,2,2,2] row_mask:0xf bank_mask:0xf\n"
      "v_mov_b32_dpp v15, v24 quad_perm:[3,3,3,3] row_mask:0xf bank_mask:0xf\n"
      "s_nop 1\n"
      "v_smfmac_f32_16x16x64_bf16 v[76:79], v[12:15], v[60:67], v1 abid:1\n"
      "v_smfmac_f32_16x16x64_bf16 v[80:83], v[12:15], v[68:75], v1 abid:1\n"
      ".Lk_rdy:\n"
      "s_waitcnt vmcnt(0)\n"
      "v_cmp_lt_u32 vcc, s67, v20\n"
      "s_cbranch_vccnz .Lk_rdy_retry\n"
      "s_nop 3\n"
      "v_cvt_pk_bf16_f32 v32, v76, v77\n"
      "v_cvt_pk_bf16_f32 v33, v78, v79\n"
      "v_cvt_pk_bf16_f32 v34, v80, v81\n"
      "v_cvt_pk_bf16_f32 v35, v82, v83\n"
      "buffer_store_dwordx4 v[32:35], v96, s[40:43], s62 offen sc1\n"
      "s_branch .Lk_next\n"
      ".Lk_p_retry:\n"
      "buffer_load_dwordx4 v[12:15], v95, s[40:43], 0 offen sc1\n"
      "buffer_load_dwordx4 v[16:19], v95, s[40:43], 0 offen offset:512 sc1\n"
      "buffer_load_dwordx2 v[28:29], v89, s[44:47], 0 offen sc1\n"
      "buffer_load_dwordx4 v[60:63], v88, s[40:43], s74 offen sc1\n"
      "buffer_load_dwordx4 v[64:67], v88, s[40:43], s74 offen offset:512 sc1\n"
      "buffer_load_dwordx4 v[68:71], v88, s[40:43], s74 offen offset:1024 sc1\n"
      "buffer_load_dwordx4 v[72:75], v88, s[40:43], s74 offen offset:1536 sc1\n"
      "s_sub_u32 s66, s66, 1\n"
      "s_cselect_b32 s67, -1, s67\n"
      "s_waitcnt vmcnt(4)\n"
      "s_branch .Lk_p_wait\n"
      ".Lk_eta_put:\n"
      "s_sub_u32 s7, s6, s19\n"
      "s_movk_i32 s68, 75\n"
      "s_add_u32 s68, s68, s7\n"
      "s_cmp_gt_i32 s7, -6\n"
      "s_cselect_b32 s68, s68, 0\n"
      "s_add_u32 s68, s68, s22\n"
      "v_mov_b32 v24, s68\n"
      "v_mov_b32 v25, s70\n"
      "buffer_store_dwordx2 v[24:25], v31, s[40:43], 0 offen sc1\n"
      "s_branch .Lk_eta_done\n"
      ".Lk_rdy_retry:\n"
      "buffer_load_dwordx2 v[20:21], v101, s[40:43], s63 offen sc1\n"
      "s_sub_u32 s66, s66, 1\n"
      "s_cselect_b32 s67, -1, s67\n"
      "s_waitcnt vmcnt(0)\n"
      "s_branch .Lk_rdy\n"
      ".Lk_z_retry:\n"
      "buffer_load_dwordx4 v[12:15], v95, s[40:43], 0 offen sc1\n"
      "buffer_load_dwordx4 v[16:19], v95, s[40:43], 0 offen offset:512 sc1\n"
      "buffer_load_dwordx2 v[20:21], v101, s[40:43], s63 offen sc1\n"
      "s_sub_u32 s66, s66, 1\n"
      "s_cselect_b32 s67, -1, s67\n"
      "s_branch .Lk_z_wait\n"
      ".p2align 12\n"
      ".Lk_red:\n"
      "s_and_b32 s69, s75, 0x1f\n"
      "s_lshl_b32 s69, s69, 6\n"
      "s_and_b32 s68, s75, 0x1\n"
      "s_lshl_b32 s68, s68, 6\n"
      "s_add_u32 s68, s68, 0x10000\n"
      "v_bfe_u32 v11, v0, 4, 2\n"
      "v_lshl_add_u32 v51, v11, 14, s68\n"
      "v_bfe_u32 v11, v0, 2, 2\n"
      "v_lshl_add_u32 v51, v11, 11, v51\n"
      "v_bfe_u32 v11, v0, 0, 2\n"
      "v_lshl_add_u32 v51, v11, 4, v51\n"
      "s_and_b32 s68, s75, 0x1e\n"
      "s_lshr_b32 s68, s68, 1\n"
      "v_bfe_u32 v11, v0, 2, 4\n"
      "v_lshl_add_u32 v52, v11, 0, s68\n"
      "v_and_b32 v52, 15, v52\n"
      "v_lshl_add_u32 v51, v52, 7, v51\n"
      "s_mov_b32 s78, 0xffffff\n"
      "s_mov_b32 s79, 0x0\n"
      "v_mov_b32 v11, 0x40000000\n"
      "v_cndmask_b32 v52, v11, v51, s[78:79]\n"
      "s_cmp_lg_u32 s62, 0\n"
      "s_cbranch_scc1 .Lk_rc_130\n"
      "s_and_b32 s68, s75, 0x1f\n"
      "s_lshl_b32 s68, s68, 5\n"
      "s_add_u32 s68, s68, 0x174000\n"
      "v_bfe_u32 v11, v0, 0, 3\n"
      "v_lshl_add_u32 v15, v11, 2, s68\n"
      "v_bfe_u32 v11, v0, 3, 1\n"
      "v_lshl_add_u32 v15, v11, 13, v15\n"
      "v_bfe_u32 v11, v0, 4, 2\n"
      "v_lshl_add_u32 v15, v11, 29, v15\n"
      "s_waitcnt vmcnt(0)\n"
      "s_barrier\n"
      "s_cmp_lg_u32 s56, 1\n"
      "s_cbranch_scc1 .Lk_rc_130\n"
      "buffer_store_dword v0, v15, s[40:43], 0 offen sc1\n"
      "s_and_b32 s68, s75, 3\n"
      "s_cbranch_scc0 .Lk_keeper\n"
      ".Lk_rc_130:\n"
      "s_cmp_lg_u32 s56, 0\n"
      "s_cbranch_scc1 .Lk_next\n"
      "s_and_b32 s68, s75, 0x18\n"
      "s_lshl_b32 s68, s68, 11\n"
      "s_and_b32 s79, s75, 0x6\n"
      "s_lshl_b32 s79, s79, 10\n"
      "s_add_u32 s68, s68, s79\n"
      "s_and_b32 s79, s75, 0x1\n"
      "s_lshl_b32 s79, s79, 6\n"
      "s_add_u32 s68, s68, s79\n"
      "s_add_u32 s68, s68, 0x88000\n"
      "v_bfe_u32 v11, v0, 2, 4\n"
      "v_lshl_add_u32 v50, v11, 7, s68\n"
      "v_bfe_u32 v11, v0, 0, 2\n"
      "v_lshl_add_u32 v50, v11, 4, v50\n"
      "v_bfe_u32 v11, v0, 0, 2\n"
      "v_lshl_add_u32 v53, v11, 4, s69\n"
      "v_bfe_u32 v11, v0, 4, 1\n"
      "v_lshl_add_u32 v53, v11, 3, v53\n"
      "v_bfe_u32 v11, v0, 0, 2\n"
      "v_lshl_add_u32 v54, v11, 4, s69\n"
      "v_bfe_u32 v11, v0, 2, 2\n"
      "v_lshl_add_u32 v54, v11, 11, v54\n"
      "v_bfe_u32 v11, v0, 5, 1\n"
      "v_lshl_add_u32 v54, v11, 13, v54\n"
      "v_bfe_u32 v11, v0, 4, 1\n"
      "v_lshl_add_u32 v54, v11, 30, v54\n"
      "s_mov_b32 s68, 1507328\n"
      "s_add_u32 s48, s14, s68\n"
      "s_addc_u32 s49, s15, 0\n"
      "s_and_b32 s49, s49, 0xffff\n"
      "s_sub_u32 s50, 0x174000, s68\n"
      "s_mov_b32 s51, 0x20000\n"
      "s_mov_b32 s52, s26\n"
      "s_and_b32 s53, s27, 0xffff\n"
      "s_mov_b32 s54, 0x400\n"
      "s_mov_b32 s55, 0x20000\n"
      "v_and_b32 v11, 0x3ff, v53\n"
      "buffer_load_dwordx2 v[8:9], v11, s[52:55], 0 offen \n"
      "s_cmp_eq_u32 s62, 0\n"
      "s_cbranch_scc1 .Lk_ro_131\n"
      "s_mov_b32 s48, s28\n"
      "s_and_b32 s49, s29, 0xffff\n"
      "s_mov_b32 s50, 0x800\n"
      "s_mov_b32 s51, 0x20000\n"
      "buffer_load_dwordx2 v[44:45], v53, s[48:51], 0 offen \n"
      ".Lk_ro_131:\n"
      "v_mov_b32 v11, 0x3f80\n"
      "v_mov_b64 v[84:85], 0\n"
      "v_mov_b64 v[86:87], 0\n"
      "v_mov_b64 v[64:65], 0\n"
      "v_mov_b64 v[66:67], 0\n"
      "v_mov_b32_dpp v84, v11 quad_perm:[0,1,2,3] row_mask:0xf bank_mask:0x5\n"
      "v_mov_b32_dpp v85, v11 quad_perm:[0,1,2,3] row_mask:0xf bank_mask:0xa\n"
      "v_mov_b32_dpp v86, v11 quad_perm:[0,1,2,3] row_mask:0xf bank_mask:0x5\n"
      "v_mov_b32_dpp v87, v11 quad_perm:[0,1,2,3] row_mask:0xf bank_mask:0xa\n"
      "s_add_u32 s19, s18, 182\n"
      "s_add_u32 s11, s18, 579\n"
      "s_cmp_eq_u32 s62, 0\n"
      "s_cselect_b32 s19, s19, s11\n"
      "s_branch .Lk_nb_137\n"
      ".Lk_na_136:\n"
      "s_sleep 4\n"
      ".Lk_nb_137:\n"
      "s_memrealtime s[6:7]\n"
      "s_waitcnt lgkmcnt(0)\n"
      "s_sub_u32 s23, s6, s19\n"
      "s_cmp_lt_i32 s23, -24\n"
      "s_cbranch_scc1 .Lk_na_136\n"
      ".Lk_un_132:\n"
      "s_memrealtime s[84:85]\n"
      "s_waitcnt lgkmcnt(2)\n"
      "s_sub_u32 s23, s6, s19\n"
      "s_cmp_lt_i32 s23, -3\n"
      "s_cbranch_scc0 .Lk_uo_133\n"
      "s_nop 4\n"
      "s_memrealtime s[6:7]\n"
      "s_waitcnt lgkmcnt(2)\n"
      "s_sub_u32 s23, s84, s19\n"
      "s_cmp_lt_i32 s23, -3\n"
      "s_cbranch_scc1 .Lk_un_132\n"
      "s_nop 4\n"
      ".Lk_uo_133:\n"
      ".Lk_uf_134:\n"
      "s_addk_i32 s23, 1\n"
      "s_cmp_lt_i32 s23, 1\n"
      "s_cbranch_scc1 .Lk_uf_134\n"
      "buffer_load_dwordx2 v[44:45], v53, s[44:47], 0 offen sc1\n"
      "buffer_load_dwordx4 v[60:63], v50, s[40:43], s74 offen sc1\n"
      "s_waitcnt lgkmcnt(0)\n"
      "s_add_u32 s20, s19, s23\n"
      "s_cmp_gt_i32 s23, 8\n"
      "s_cselect_b32 s7, s23, 0\n"
      "s_add_u32 s18, s18, s7\n"
      ".Lk_rp_wait:\n"
      "s_waitcnt vmcnt(0)\n"
      "v_max3_u32 v11, v44, v45, v60\n"
      "v_max3_u32 v11, v11, v61, v62\n"
      "v_max_u32 v11, v11, v63\n"
      "v_cmp_lt_u32 vcc, s67, v11\n"
      "s_cbranch_vccnz .Lk_rp_retry\n"
      "v_lshlrev_b32 v88, 16, v44\n"
      "v_and_b32 v89, 0xffff0000, v44\n"
      "v_lshlrev_b32 v68, 16, v8\n"
      "v_and_b32 v69, 0xffff0000, v8\n"
      "v_add_f32 v88, v88, v68\n"
      "v_add_f32 v89, v89, v69\n"
      "v_lshlrev_b32 v90, 16, v45\n"
      "v_and_b32 v91, 0xffff0000, v45\n"
      "v_lshlrev_b32 v68, 16, v9\n"
      "v_and_b32 v69, 0xffff0000, v9\n"
      "v_add_f32 v90, v90, v68\n"
      "v_add_f32 v91, v91, v69\n"
      "v_mov_b64 v[92:93], 0\n"
      "v_mov_b64 v[94:95], 0\n"
      "s_nop 0\n"
      "v_mov_b32_dpp v92, v88 quad_perm:[0,1,2,3] row_mask:0xf bank_mask:0x1\n"
      "v_mov_b32_dpp v93, v89 quad_perm:[0,1,2,3] row_mask:0xf bank_mask:0x1\n"
      "v_mov_b32_dpp v94, v90 quad_perm:[0,1,2,3] row_mask:0xf bank_mask:0x1\n"
      "v_mov_b32_dpp v95, v91 quad_perm:[0,1,2,3] row_mask:0xf bank_mask:0x1\n"
      "s_nop 1\n"
      "v_smfmac_f32_16x16x64_bf16 v[92:95], v[84:87], v[60:67], v1 abid:1\n"
      "s_cmp_lg_u32 s62, 0\n"
      "s_cbranch_scc1 .Lk_re_138\n"
      "s_mov_b32 s68, 106496\n"
      "s_and_b32 s69, s57, 0x3\n"
      "s_lshl_b32 s69, s69, 5\n"
      "v_bfe_u32 v11, v0, 5, 1\n"
      "v_lshl_add_u32 v49, v11, 12, s68\n"
      "v_bfe_u32 v11, v0, 3, 1\n"
      "v_lshl_add_u32 v49, v11, 9, v49\n"
      "v_bfe_u32 v11, v0, 3, 1\n"
      "v_lshl_add_u32 v49, v11, 6, v49\n"
      "v_bfe_u32 v11, v0, 4, 1\n"
      "v_lshl_add_u32 v49, v11, 8, v49\n"
      "v_bfe_u32 v11, v0, 4, 1\n"
      "v_lshl_add_u32 v49, v11, 5, v49\n"
      "v_bfe_u32 v11, v0, 2, 1\n"
      "v_lshl_add_u32 v49, v11, 7, v49\n"
      "v_bfe_u32 v11, v0, 0, 2\n"
      "v_lshl_add_u32 v49, v11, 3, v49\n"
      "v_xor_b32 v49, s69, v49\n"
      "ds_read_b64_tr_b16 v[28:29], v49 offset:0\n"
      "ds_read_b64_tr_b16 v[30:31], v49 offset:1024\n"
      "ds_read_b64_tr_b16 v[32:33], v49 offset:8192\n"
      "ds_read_b64_tr_b16 v[34:35], v49 offset:9216\n"
      "ds_read_b64_tr_b16 v[36:37], v49 offset:2048\n"
      "ds_read_b64_tr_b16 v[38:39], v49 offset:3072\n"
      "ds_read_b64_tr_b16 v[40:41], v49 offset:10240\n"
      "ds_read_b64_tr_b16 v[42:43], v49 offset:11264\n"
      "s_and_b32 s68, s57, 0xc0\n"
      "s_lshl_b32 s68, s68, 2\n"
      "s_and_b32 s79, s57, 0x1\n"
      "s_lshl_b32 s79, s79, 11\n"
      "s_add_u32 s68, s68, s79\n"
      "v_bfe_u32 v11, v0, 2, 1\n"
      "v_lshl_add_u32 v46, v11, 10, s68\n"
      "v_bfe_u32 v11, v0, 4, 2\n"
      "v_lshl_add_u32 v46, v11, 6, v46\n"
      "v_bfe_u32 v11, v0, 0, 2\n"
      "v_lshl_add_u32 v46, v11, 4, v46\n"
      "s_and_b32 s68, s57, 0x80\n"
      "s_lshl_b32 s68, s68, 3\n"
      "s_and_b32 s79, s57, 0x1\n"
      "s_lshl_b32 s79, s79, 11\n"
      "s_add_u32 s68, s68, s79\n"
      "s_add_u32 s68, s68, 0x1000\n"
      "v_lshl_add_u32 v47, v0, 4, s68\n"
      "s_and_b32 s68, s57, 0x1c\n"
      "s_lshl_b32 s68, s68, 13\n"
      "s_and_b32 s79, s57, 0x200\n"
      "s_lshl_b32 s79, s79, 5\n"
      "s_add_u32 s68, s68, s79\n"
      "s_and_b32 s79, s57, 0x100\n"
      "s_lshl_b32 s79, s79, 4\n"
      "s_add_u32 s68, s68, s79\n"
      "s_and_b32 s79, s57, 0x20\n"
      "s_lshl_b32 s79, s79, 5\n"
      "s_add_u32 s68, s68, s79\n"
      "s_and_b32 s79, s57, 0x3\n"
      "s_lshl_b32 s79, s79, 8\n"
      "s_add_u32 s68, s68, s79\n"
      "s_and_b32 s79, s57, 0x40\n"
      "s_lshl_b32 s79, s79, 1\n"
      "s_add_u32 s68, s68, s79\n"
      "v_bfe_u32 v11, v0, 0, 4\n"
      "v_lshl_add_u32 v48, v11, 2, s68\n"
      "v_bfe_u32 v11, v0, 4, 1\n"
      "v_lshl_add_u32 v48, v11, 11, v48\n"
      "v_bfe_u32 v11, v0, 5, 1\n"
      "v_lshl_add_u32 v48, v11, 13, v48\n"
      "s_and_b32 s68, s57, 0x80\n"
      "s_lshr_b32 s68, s68, 1\n"
      "s_and_b32 s79, s57, 0x80\n"
      "s_lshr_b32 s79, s79, 4\n"
      "s_add_u32 s68, s68, s79\n"
      "v_xor_b32 v48, s68, v48\n"
      "s_mov_b32 s78, 0xffffffff\n"
      "s_mov_b32 s79, 0x0\n"
      "v_mov_b32 v11, 0x40000000\n"
      "v_cndmask_b32 v48, v11, v48, s[78:79]\n"
      "s_and_b32 s68, s57, 0x1c\n"
      "s_lshl_b32 s68, s68, 13\n"
      "s_and_b32 s79, s57, 0x200\n"
      "s_lshl_b32 s79, s79, 5\n"
      "s_add_u32 s68, s68, s79\n"
      "s_and_b32 s79, s57, 0x100\n"
      "s_lshl_b32 s79, s79, 4\n"
      "s_add_u32 s68, s68, s79\n"
      "s_and_b32 s79, s57, 0x80\n"
      "s_lshl_b32 s79, s79, 4\n"
      "s_add_u32 s68, s68, s79\n"
      "v_bfe_u32 v11, v0, 3, 3\n"
      "v_lshl_add_u32 v96, v11, 8, s68\n"
      "v_bfe_u32 v11, v0, 0, 1\n"
      "v_lshl_add_u32 v96, v11, 6, v96\n"
      "v_bfe_u32 v11, v0, 1, 1\n"
      "v_lshl_add_u32 v96, v11, 5, v96\n"
      "v_bfe_u32 v11, v0, 2, 1\n"
      "v_lshl_add_u32 v96, v11, 4, v96\n"
      "s_cmp_eq_u32 s18, s22\n"
      "s_cbranch_scc1 .Lk_re_138\n"
      "s_and_b32 s68, s57, 0x1c\n"
      "s_lshl_b32 s68, s68, 5\n"
      "s_add_u32 s68, s68, 0x174400\n"
      "v_mov_b32 v12, s68\n"
      ".Lk_tick2:\n"
      "buffer_load_dword v13, v12, s[40:43], s63 offen sc1\n"
      "s_sub_u32 s66, s66, 1\n"
      "s_cselect_b32 s67, -1, s67\n"
      "s_waitcnt vmcnt(0)\n"
      "v_cmp_lt_u32 vcc, s67, v13\n"
      "s_cbranch_vccnz .Lk_tick2_retry\n"
      "v_readfirstlane_b32 s68, v13\n"
      "s_nop 0\n"
      "s_sub_u32 s68, s68, s22\n"
      "s_bfe_i32 s68, s68, 0x1f0000\n"
      "s_min_i32 s68, s68, 4096\n"
      "s_add_u32 s18, s22, s68\n"
      ".Lk_re_138:\n"
      "s_add_u32 s19, s18, 292\n"
      "s_add_u32 s11, s18, 685\n"
      "s_cmp_eq_u32 s62, 0\n"
      "s_cselect_b32 s19, s19, s11\n"
      "s_nop 0\n"
      "s_branch .Lk_nb_144\n"
      ".Lk_na_143:\n"
      "s_sleep 4\n"
      ".Lk_nb_144:\n"
      "s_memrealtime s[6:7]\n"
      "s_waitcnt lgkmcnt(0)\n"
      "s_sub_u32 s23, s6, s19\n"
      "s_cmp_lt_i32 s23, -24\n"
      "s_cbranch_scc1 .Lk_na_143\n"
      ".Lk_un_139:\n"
      "s_memrealtime s[84:85]\n"
      "s_waitcnt lgkmcnt(2)\n"
      "s_sub_u32 s23, s6, s19\n"
      "s_cmp_lt_i32 s23, -3\n"
      "s_cbranch_scc0 .Lk_uo_140\n"
      "s_nop 4\n"
      "s_memrealtime s[6:7]\n"
      "s_waitcnt lgkmcnt(2)\n"
      "s_sub_u32 s23, s84, s19\n"
      "s_cmp_lt_i32 s23, -3\n"
      "s_cbranch_scc1 .Lk_un_139\n"
      "s_nop 4\n"
      ".Lk_uo_140:\n"
      ".Lk_uf_141:\n"
      "s_addk_i32 s23, 1\n"
      "s_cmp_lt_i32 s23, 1\n"
      "s_cbranch_scc1 .Lk_uf_141\n"
      "s_add_u32 s68, s74, 0\n"
      "buffer_load_dwordx4 v[60:63], v51, s[40:43], s68 offen sc1\n"
      "s_add_u32 s68, s74, 65536\n"
      "buffer_load_dwordx4 v[64:67], v51, s[40:43], s68 offen sc1\n"
      "s_add_u32 s68, s74, 131072\n"
      "buffer_load_dwordx4 v[68:71], v51, s[40:43], s68 offen sc1\n"
      "s_add_u32 s68, s74, 196608\n"
      "buffer_load_dwordx4 v[72:75], v51, s[40:43], s68 offen sc1\n"
      "s_add_u32 s68, s74, 262144\n"
      "buffer_load_dwordx4 v[76:79], v51, s[40:43], s68 offen sc1\n"
      "s_add_u32 s68, s74, 327680\n"
      "buffer_load_dwordx4 v[80:83], v52, s[40:43], s68 offen sc1\n"
      "s_waitcnt lgkmcnt(0)\n"
      "s_add_u32 s20, s19, s23\n"
      "s_cmp_gt_i32 s23, 8\n"
      "s_cselect_b32 s7, s23, 0\n"
      "s_add_u32 s18, s18, s7\n"
      ".Lk_f_wait:\n"
      "s_cmp_lg_u32 s62, 0\n"
      "v_mov_b64 v[88:89], v[92:93]\n"
      "v_mov_b64 v[90:91], v[94:95]\n"
      "s_waitcnt vmcnt(5)\n"
      "v_max3_u32 v11, v60, v61, v62\n"
      "v_max_u32 v11, v11, v63\n"
      "s_waitcnt vmcnt(4)\n"
      "v_smfmac_f32_16x16x64_bf16 v[88:91], v[84:87], v[60:67], v1 abid:1\n"
      "v_max3_u32 v11, v11, v64, v65\n"
      "v_max3_u32 v11, v11, v66, v67\n"
      "s_waitcnt vmcnt(3)\n"
      "v_max3_u32 v11, v11, v68, v69\n"
      "v_max3_u32 v11, v11, v70, v71\n"
      "s_waitcnt vmcnt(2)\n"
      "v_smfmac_f32_16x16x64_bf16 v[88:91], v[84:87], v[68:75], v1 abid:1\n"
      "v_max3_u32 v11, v11, v72, v73\n"
      "v_max3_u32 v11, v11, v74, v75\n"
      "s_waitcnt vmcnt(1)\n"
      "v_max3_u32 v11, v11, v76, v77\n"
      "v_max3_u32 v11, v11, v78, v79\n"
      "s_waitcnt vmcnt(0)\n"
      "v_smfmac_f32_16x16x64_bf16 v[88:91], v[84:87], v[76:83], v1 abid:1\n"
      "v_max3_u32 v11, v11, v80, v81\n"
      "v_max3_u32 v11, v11, v82, v83\n"
      "v_cmp_lt_u32 vcc, s67, v11\n"
      "s_cbranch_vccnz .Lk_f_retry\n"
      "s_nop 3\n"
      "v_add_f32_dpp v88, v88, v88 row_ror:4 row_mask:0xf bank_mask:0xf\n"
      "v_add_f32_dpp v89, v89, v89 row_ror:4 row_mask:0xf bank_mask:0xf\n"
      "v_add_f32_dpp v90, v90, v90 row_ror:4 row_mask:0xf bank_mask:0xf\n"
      "v_add_f32_dpp v91, v91, v91 row_ror:4 row_mask:0xf bank_mask:0xf\n"
      "v_add_f32_dpp v88, v88, v88 row_ror:8 row_mask:0xf bank_mask:0xf\n"
      "v_add_f32_dpp v89, v89, v89 row_ror:8 row_mask:0xf bank_mask:0xf\n"
      "v_add_f32_dpp v90, v90, v90 row_ror:8 row_mask:0xf bank_mask:0xf\n"
      "v_add_f32_dpp v91, v91, v91 row_ror:8 row_mask:0xf bank_mask:0xf\n"
      "v_cvt_pk_bf16_f32 v92, v88, v89\n"
      "v_cvt_pk_bf16_f32 v94, v88, v89\n"
      "v_cvt_pk_bf16_f32 v93, v90, v91\n"
      "v_cvt_pk_bf16_f32 v95, v90, v91\n"
      "v_permlane16_swap_b32 v92, v94\n"
      "s_nop 0\n"
      "v_permlane16_swap_b32 v93, v95\n"
      "buffer_store_dwordx4 v[92:95], v54, s[48:51], 0 offen sc1\n"
      "s_cmp_eq_u32 s62, 0\n"
      "s_cbranch_scc1 .Lk_s5\n"
      "s_branch .Lk_exit\n"
      ".Lk_keeper:\n"
      "s_mov_b32 s68, 106496\n"
      "s_and_b32 s69, s57, 0x3\n"
      "s_lshl_b32 s69, s69, 5\n"
      "v_bfe_u32 v11, v0, 5, 1\n"
      "v_lshl_add_u32 v49, v11, 12, s68\n"
      "v_bfe_u32 v11, v0, 3, 1\n"
      "v_lshl_add_u32 v49, v11, 9, v49\n"
      "v_bfe_u32 v11, v0, 3, 1\n"
      "v_lshl_add_u32 v49, v11, 6, v49\n"
      "v_bfe_u32 v11, v0, 4, 1\n"
      "v_lshl_add_u32 v49, v11, 8, v49\n"
      "v_bfe_u32 v11, v0, 4, 1\n"
      "v_lshl_add_u32 v49, v11, 5, v49\n"
      "v_bfe_u32 v11, v0, 2, 1\n"
      "v_lshl_add_u32 v49, v11, 7, v49\n"
      "v_bfe_u32 v11, v0, 0, 2\n"
      "v_lshl_add_u32 v49, v11, 3, v49\n"
      "v_xor_b32 v49, s69, v49\n"
      "ds_read_b64_tr_b16 v[28:29], v49 offset:0\n"
      "ds_read_b64_tr_b16 v[30:31], v49 offset:1024\n"
      "ds_read_b64_tr_b16 v[32:33], v49 offset:8192\n"
      "ds_read_b64_tr_b16 v[34:35], v49 offset:9216\n"
      "ds_read_b64_tr_b16 v[36:37], v49 offset:2048\n"
      "ds_read_b64_tr_b16 v[38:39], v49 offset:3072\n"
      "ds_read_b64_tr_b16 v[40:41], v49 offset:10240\n"
      "ds_read_b64_tr_b16 v[42:43], v49 offset:11264\n"
      "s_and_b32 s68, s57, 0xc0\n"
      "s_lshl_b32 s68, s68, 2\n"
      "s_and_b32 s79, s57, 0x1\n"
      "s_lshl_b32 s79, s79, 11\n"
      "s_add_u32 s68, s68, s79\n"
      "v_bfe_u32 v11, v0, 2, 1\n"
      "v_lshl_add_u32 v46, v11, 10, s68\n"
      "v_bfe_u32 v11, v0, 4, 2\n"
      "v_lshl_add_u32 v46, v11, 6, v46\n"
      "v_bfe_u32 v11, v0, 0, 2\n"
      "v_lshl_add_u32 v46, v11, 4, v46\n"
      "s_and_b32 s68, s57, 0x80\n"
      "s_lshl_b32 s68, s68, 3\n"
      "s_and_b32 s79, s57, 0x1\n"
      "s_lshl_b32 s79, s79, 11\n"
      "s_add_u32 s68, s68, s79\n"
      "s_add_u32 s68, s68, 0x1000\n"
      "v_lshl_add_u32 v47, v0, 4, s68\n"
      "s_and_b32 s68, s57, 0x1c\n"
      "s_lshl_b32 s68, s68, 13\n"
      "s_and_b32 s79, s57, 0x200\n"
      "s_lshl_b32 s79, s79, 5\n"
      "s_add_u32 s68, s68, s79\n"
      "s_and_b32 s79, s57, 0x100\n"
      "s_lshl_b32 s79, s79, 4\n"
      "s_add_u32 s68, s68, s79\n"
      "s_and_b32 s79, s57, 0x20\n"
      "s_lshl_b32 s79, s79, 5\n"
      "s_add_u32 s68, s68, s79\n"
      "s_and_b32 s79, s57, 0x3\n"
      "s_lshl_b32 s79, s79, 8\n"
      "s_add_u32 s68, s68, s79\n"
      "s_and_b32 s79, s57, 0x40\n"
      "s_lshl_b32 s79, s79, 1\n"
      "s_add_u32 s68, s68, s79\n"
      "v_bfe_u32 v11, v0, 0, 4\n"
      "v_lshl_add_u32 v48, v11, 2, s68\n"
      "v_bfe_u32 v11, v0, 4, 1\n"
      "v_lshl_add_u32 v48, v11, 11, v48\n"
      "v_bfe_u32 v11, v0, 5, 1\n"
      "v_lshl_add_u32 v48, v11, 13, v48\n"
      "s_and_b32 s68, s57, 0x80\n"
      "s_lshr_b32 s68, s68, 1\n"
      "s_and_b32 s79, s57, 0x80\n"
      "s_lshr_b32 s79, s79, 4\n"
      "s_add_u32 s68, s68, s79\n"
      "v_xor_b32 v48, s68, v48\n"
      "s_mov_b32 s78, 0xffffffff\n"
      "s_mov_b32 s79, 0x0\n"
      "v_mov_b32 v11, 0x40000000\n"
      "v_cndmask_b32 v48, v11, v48, s[78:79]\n"
      "s_and_b32 s68, s57, 0x1c\n"
      "s_lshl_b32 s68, s68, 13\n"
      "s_and_b32 s79, s57, 0x200\n"
      "s_lshl_b32 s79, s79, 5\n"
      "s_add_u32 s68, s68, s79\n"
      "s_and_b32 s79, s57, 0x100\n"
      "s_lshl_b32 s79, s79, 4\n"
      "s_add_u32 s68, s68, s79\n"
      "s_and_b32 s79, s57, 0x80\n"
      "s_lshl_b32 s79, s79, 4\n"
      "s_add_u32 s68, s68, s79\n"
      "v_bfe_u32 v11, v0, 3, 3\n"
      "v_lshl_add_u32 v96, v11, 8, s68\n"
      "v_bfe_u32 v11, v0, 0, 1\n"
      "v_lshl_add_u32 v96, v11, 6, v96\n"
      "v_bfe_u32 v11, v0, 1, 1\n"
      "v_lshl_add_u32 v96, v11, 5, v96\n"
      "v_bfe_u32 v11, v0, 2, 1\n"
      "v_lshl_add_u32 v96, v11, 4, v96\n"
      "s_add_u32 s68, s63, 1900544\n"
      "v_lshl_add_u32 v12, v0, 7, s68\n"
      "v_min_u32 v13, 23, v0\n"
      "s_add_u32 s68, s68, 16384\n"
      "v_lshl_add_u32 v13, v13, 7, s68\n"
      "s_and_b32 s68, s57, 0x1c\n"
      "s_lshl_b32 s68, s68, 5\n"
      "s_add_u32 s68, s68, 0x174400\n"
      "v_bfe_u32 v11, v0, 0, 5\n"
      "v_lshl_add_u32 v19, v11, 2, s68\n"
      "v_bfe_u32 v11, v0, 5, 1\n"
      "v_lshl_add_u32 v19, v11, 30, v19\n"
      "s_add_u32 s19, s18, 238\n"
      "s_branch .Lk_nb_150\n"
      ".Lk_na_149:\n"
      "s_sleep 4\n"
      ".Lk_nb_150:\n"
      "s_memrealtime s[6:7]\n"
      "s_waitcnt lgkmcnt(0)\n"
      "s_sub_u32 s23, s6, s19\n"
      "s_cmp_lt_i32 s23, -24\n"
      "s_cbranch_scc1 .Lk_na_149\n"
      ".Lk_un_145:\n"
      "s_memrealtime s[84:85]\n"
      "s_waitcnt lgkmcnt(2)\n"
      "s_sub_u32 s23, s6, s19\n"
      "s_cmp_lt_i32 s23, -3\n"
      "s_cbranch_scc0 .Lk_uo_146\n"
      "s_nop 4\n"
      "s_memrealtime s[6:7]\n"
      "s_waitcnt lgkmcnt(2)\n"
      "s_sub_u32 s23, s84, s19\n"
      "s_cmp_lt_i32 s23, -3\n"
      "s_cbranch_scc1 .Lk_un_145\n"
      "s_nop 4\n"
      ".Lk_uo_146:\n"
      ".Lk_uf_147:\n"
      "s_addk_i32 s23, 1\n"
      "s_cmp_lt_i32 s23, 1\n"
      "s_cbranch_scc1 .Lk_uf_147\n"
      ".Lk_eta:\n"
      "buffer_load_dwordx2 v[14:15], v12, s[40:43], 0 offen sc1\n"
      "buffer_load_dwordx2 v[16:17], v13, s[40:43], 0 offen sc1\n"
      "s_sub_u32 s66, s66, 1\n"
      "s_cselect_b32 s67, -1, s67\n"
      "s_bfe_i32 s69, s67, 0x10000\n"
      "s_waitcnt vmcnt(0)\n"
      "v_xor_b32 v11, s70, v15\n"
      "v_subrev_u32 v14, s22, v14\n"
      "v_subrev_u32 v16, s22, v16\n"
      "v_bitop3_b32 v11, v17, s70, v11 bitop3:0xbe\n"
      "v_max_i32 v14, v14, v16\n"
      "v_cmp_lt_u32 vcc, s69, v11\n"
      "s_cbranch_vccnz .Lk_eta_retry\n"
      "v_cmp_lt_i32 vcc, 4, v14\n"
      "s_mov_b32 s68, 0\n"
      "s_cbranch_vccz .Lk_eta_plan\n"
      "v_max_i32_dpp v14, v14, v14 row_ror:8 row_mask:0xf bank_mask:0xf\n"
      "s_nop 1\n"
      "v_max_i32_dpp v14, v14, v14 row_ror:4 row_mask:0xf bank_mask:0xf\n"
      "s_nop 1\n"
      "v_max_i32_dpp v14, v14, v14 row_ror:2 row_mask:0xf bank_mask:0xf\n"
      "s_nop 1\n"
      "v_max_i32_dpp v14, v14, v14 row_ror:1 row_mask:0xf bank_mask:0xf\n"
      "s_nop 1\n"
      "v_max_i32_dpp v14, v14, v14 row_bcast:15 row_mask:0xa bank_mask:0xf\n"
      "s_nop 1\n"
      "v_max_i32_dpp v14, v14, v14 row_bcast:31 row_mask:0xc bank_mask:0xf\n"
      "s_nop 0\n"
      "v_readlane_b32 s68, v14, 63\n"
      "s_nop 0\n"
      "s_min_i32 s68, s68, 4096\n"
      ".Lk_eta_plan:\n"
      "s_add_u32 s18, s22, s68\n"
      "s_and_b32 s68, s18, 0x7fffffff\n"
      "v_mov_b32 v18, s68\n"
      "buffer_store_dword v18, v19, s[40:43], s63 offen sc1\n"
      "s_branch .Lk_s5\n"
      ".Lk_tick2_retry:\n"
      "s_branch .Lk_tick2\n"
      ".Lk_eta_retry:\n"
      "s_branch .Lk_eta\n"
      ".Lk_rp_retry:\n"
      "buffer_load_dwordx2 v[44:45], v53, s[44:47], 0 offen sc1\n"
      "buffer_load_dwordx4 v[60:63], v50, s[40:43], s74 offen sc1\n"
      "s_memrealtime s[6:7]\n"
      "s_sub_u32 s66, s66, 1\n"
      "s_cselect_b32 s67, -1, s67\n"
      "s_waitcnt lgkmcnt(0)\n"
      "s_sub_u32 s7, s6, s20\n"
      "s_mov_b32 s20, s6\n"
      "s_add_u32 s18, s18, s7\n"
      "s_add_u32 s19, s19, s7\n"
      "s_branch .Lk_rp_wait\n"
      ".Lk_f_retry:\n"
      "s_add_u32 s68, s74, 0\n"
      "buffer_load_dwordx4 v[60:63], v51, s[40:43], s68 offen sc1\n"
      "s_add_u32 s68, s74, 65536\n"
      "buffer_load_dwordx4 v[64:67], v51, s[40:43], s68 offen sc1\n"
      "s_add_u32 s68, s74, 131072\n"
      "buffer_load_dwordx4 v[68:71], v51, s[40:43], s68 offen sc1\n"
      "s_add_u32 s68, s74, 196608\n"
      "buffer_load_dwordx4 v[72:75], v51, s[40:43], s68 offen sc1\n"
      "s_add_u32 s68, s74, 262144\n"
      "buffer_load_dwordx4 v[76:79], v51, s[40:43], s68 offen sc1\n"
      "s_add_u32 s68, s74, 327680\n"
      "buffer_load_dwordx4 v[80:83], v52, s[40:43], s68 offen sc1\n"
      "s_sub_u32 s66, s66, 1\n"
      "s_cselect_b32 s67, -1, s67\n"
      "s_nop 2\n"
      "s_branch .Lk_f_wait\n"
      ".Lk_tick_retry:\n"
      "s_branch .Lk_tick\n"
      ".Lk_x_retry:\n"
      "buffer_load_dwordx4 v[12:15], v46, s[44:47], 0 offen sc1\n"
      "buffer_load_dwordx4 v[24:27], v47, s[44:47], 0 offen sc1\n"
      "s_sub_u32 s66, s66, 1\n"
      "s_cselect_b32 s67, -1, s67\n"
      "s_nop 4\n"
      "s_branch .Lk_x_wait\n"
      ".Lk_xs_retry:\n"
      "buffer_load_dwordx4 v[24:27], v47, s[44:47], 0 offen sc1\n"
      "s_sub_u32 s66, s66, 1\n"
      "s_cselect_b32 s67, -1, s67\n"
      "s_waitcnt vmcnt(0)\n"
      "s_branch .Lk_xs_chk\n"
      ".Lk_q_retry:\n"
      "buffer_load_dwordx4 v[88:91], v96, s[40:43], s81 offen sc1\n"
      "buffer_load_dwordx4 v[92:95], v96, s[40:43], s81 offen offset:128 sc1\n"
      "s_memrealtime s[6:7]\n"
      "s_sub_u32 s66, s66, 1\n"
      "s_cselect_b32 s67, -1, s67\n"
      "s_waitcnt lgkmcnt(0)\n"
      "s_sub_u32 s7, s6, s20\n"
      "s_mov_b32 s20, s6\n"
      "s_add_u32 s18, s18, s7\n"
      "s_add_u32 s19, s19, s7\n"
      "s_branch .Lk_q_wait\n"
      ".Lk_exit:\n"
      "s_endpgm\n"
      ".Lk_end:\n"
      :
      : "s"(__builtin_amdgcn_kernarg_segment_ptr()), "s"(blockIdx.x), "{v0}"(threadIdx.x),
        "s"((unsigned)(uintptr_t)(__attribute__((address_space(3))) unsigned char*)lds)
      :
        "v1", "v2", "v3", "v4", "v5", "v6", "v7", "v8", "v9", "v10", "v11", "v12", "v13", "v14", "v15", "v16",
        "v17", "v18", "v19", "v20", "v21", "v22", "v23", "v24", "v25", "v26", "v27", "v28", "v29", "v30", "v31", "v32",
        "v33", "v34", "v35", "v36", "v37", "v38", "v39", "v40", "v41", "v42", "v43", "v44", "v45", "v46", "v47", "v48",
        "v49", "v50", "v51", "v52", "v53", "v54", "v55", "v56", "v57", "v58", "v59", "v60", "v61", "v62", "v63", "v64",
        "v65", "v66", "v67", "v68", "v69", "v70", "v71", "v72", "v73", "v74", "v75", "v76", "v77", "v78", "v79", "v80",
        "v81", "v82", "v83", "v84", "v85", "v86", "v87", "v88", "v89", "v90", "v91", "v92", "v93", "v94", "v95", "v96",
        "v97", "v98", "v99", "v100", "v101", "v102", "v103", "v104", "v105", "v106", "v107", "v108", "v109", "v110", "v111", "v112",
        "v113", "v114", "v115", "v116", "v117", "v118", "v119", "v120", "v121", "v122", "v123", "v124", "v125", "v126", "v127", "v128",
        "v129", "v130", "v131", "v132", "v133", "v134", "v135", "v136", "v137", "v138", "v139", "v140", "v141", "v142", "v143", "v144",
        "v145", "v146", "v147", "v148", "v149", "v150", "v151", "v152", "v153", "v154", "v155", "v156", "v157", "v158", "v159", "v160",
        "v161", "v162", "v163", "v164", "v165", "v166", "v167", "v168", "v169", "v170", "v171", "v172", "v173", "v174", "v175", "v176",
        "v177", "v178", "v179", "v180", "v181", "v182", "v183", "v184", "v185", "v186", "v187", "v188", "v189", "v190", "v191", "v192",
        "v193", "v194", "v195", "v196", "v197", "v198", "v199", "v200", "v201", "v202", "v203", "v204", "v205", "v206", "v207", "v208",
        "v209", "v210", "v211", "v212", "v213", "v214", "v215", "v216", "v217", "v218", "v219", "v220", "v221", "v222", "v223", "v224",
        "v225", "v226", "v227", "v228", "v229", "v230", "v231", "v232", "v233", "v234", "v235", "v236", "v237", "v238", "v239", "v240",
        "v241", "v242", "v243", "v244", "v245", "v246", "v247", "v248", "v249", "v250", "v251", "v252", "v253", "v254", "v255", "a0",
        "a1", "a2", "a3", "a4", "a5", "a6", "a7", "a8", "a9", "a10", "a11", "a12", "a13", "a14", "a15", "a16",
        "a17", "a18", "a19", "a20", "a21", "a22", "a23", "a24", "a25", "a26", "a27", "a28", "a29", "a30", "a31", "a32",
        "a33", "a34", "a35", "a36", "a37", "a38", "a39", "a40", "a41", "a42", "a43", "a44", "a45", "a46", "a47", "a48",
        "a49", "a50", "a51", "a52", "a53", "a54", "a55", "a56", "a57", "a58", "a59", "a60", "a61", "a62", "a63", "a64",
        "a65", "a66", "a67", "a68", "a69", "a70", "a71", "a72", "a73", "a74", "a75", "a76", "a77", "a78", "a79", "a80",
        "a81", "a82", "a83", "a84", "a85", "a86", "a87", "a88", "a89", "a90", "a91", "a92", "a93", "a94", "a95", "a96",
        "a97", "a98", "a99", "a100", "a101", "a102", "a103", "a104", "a105", "a106", "a107", "a108", "a109", "a110", "a111", "a112",
        "a113", "a114", "a115", "a116", "a117", "a118", "a119", "a120", "a121", "a122", "a123", "a124", "a125", "a126", "a127", "a128",
        "a129", "a130", "a131", "a132", "a133", "a134", "a135", "a136", "a137", "a138", "a139", "a140", "a141", "a142", "a143", "a144",
        "a145", "a146", "a147", "a148", "a149", "a150", "a151", "a152", "a153", "a154", "a155", "a156", "a157", "a158", "a159", "a160",
        "a161", "a162", "a163", "a164", "a165", "a166", "a167", "a168", "a169", "a170", "a171", "a172", "a173", "a174", "a175", "a176",
        "a177", "a178", "a179", "a180", "a181", "a182", "a183", "a184", "a185", "a186", "a187", "a188", "a189", "a190", "a191", "a192",
        "a193", "a194", "a195", "a196", "a197", "a198", "a199", "a200", "a201", "a202", "a203", "a204", "a205", "a206", "a207", "a208",
        "a209", "a210", "a211", "a212", "a213", "a214", "a215", "a216", "a217", "a218", "a219", "a220", "a221", "a222", "a223", "a224",
        "a225", "a226", "a227", "a228", "a229", "a230", "a231", "a232", "a233", "a234", "a235", "a236", "a237", "a238", "a239", "a240",
        "a241", "a242", "a243", "a244", "a245", "a246", "a247", "a248", "a249", "a250", "a251", "a252", "a253", "a254", "a255", "s4",
        "s5", "s6", "s7", "s8", "s9", "s10", "s11", "s12", "s13", "s14", "s15", "s16", "s17", "s18", "s19", "s20",
        "s21", "s22", "s23", "s24", "s25", "s26", "s27", "s28", "s29", "s30", "s31", "s32", "s33", "s34", "s35", "s36",
        "s37", "s38", "s39", "s40", "s41", "s42", "s43", "s44", "s45", "s46", "s47", "s48", "s49", "s50", "s51", "s52",
        "s53", "s54", "s55", "s56", "s57", "s58", "s59", "s60", "s61", "s62", "s63", "s64", "s65", "s66", "s67", "s68",
        "s69", "s70", "s71", "s72", "s73", "s74", "s75", "s76", "s77", "s78", "s79", "s80", "s81", "s82", "s83", "s84",
        "s85", "s86", "s87", "s88", "s89", "s90", "s91", "s92", "s93", "s94", "s95", "s96", "s97", "s98", "s99", "s100",
        "s101", "vcc", "memory"
  );
}

extern "C" void kernel_launch(void* const* d_in, const int* in_sizes, int n_in, void* d_out, int out_size,
                              void* d_ws, size_t ws_size, hipStream_t stream) {
  Args a;
  a.x = d_in[0]; a.k = d_in[1]; a.v = d_in[2]; a.wq = d_in[3]; a.wo = d_in[4];
  a.w1 = d_in[6]; a.b1 = d_in[7]; a.w2 = d_in[8]; a.b2 = d_in[9];
  a.out = d_out;
  a.ws = d_ws;
  a.dbg = d_ws;
  decode_block_kernel<<<256, 256, 0, stream>>>(a);
}
```
